```python
import torch

def _softmax(x, axis=-1):
    return torch.softmax(x, dim=axis)
def _gelu(x):
    return torch.nn.functional.gelu(x, approximate="tanh")
def _silu(x):
    return torch.nn.functional.silu(x)
def _sigmoid(x):
    return torch.sigmoid(x)
def _layernorm(x, axis=-1, eps=1e-5):
    m = x.mean(dim=axis, keepdim=True)
    v = x.var(dim=axis, keepdim=True, unbiased=False)
    return (x - m) / torch.sqrt(v + eps)
def _scatter_add(tgt, idx, src, axis=0):
    return tgt.clone().index_add_(axis, idx, src)
def _segment_sum(src, seg, n):
    out = torch.zeros((n,) + src.shape[1:], dtype=src.dtype)
    return out.index_add_(0, seg, src)
def _pad(x, pw):
    flat = tuple(v for pair in reversed(pw) for v in pair)
    return torch.nn.functional.pad(x, flat)

kernel_name = 'standalone__glu_gated_swiglu__decode'

def setup_inputs():

    _w = (lambda a: (lambda t: t.to(torch.bfloat16) if t.is_floating_point() else t)(torch.as_tensor(a)))
    B, S, D, F = 8, 1, 1024, 3584
    return {
        "x": _w((lambda *s: torch.randn(*s, dtype=torch.bfloat16) * 0.05)(B, S, D)),
        "w1": _w((lambda *s: torch.randn(*s, dtype=torch.bfloat16) * 0.05)(D, F) * (2.0 / D) ** 0.5),
        "w3": _w((lambda *s: torch.randn(*s, dtype=torch.bfloat16) * 0.05)(D, F) * (2.0 / D) ** 0.5),
        "w2": _w((lambda *s: torch.randn(*s, dtype=torch.bfloat16) * 0.05)(F, D) * (2.0 / F) ** 0.5),
    }

def reference(x, w1, w3, w2):
    gate = torch.einsum("bsd,df->bsf", x, w1)
    up = torch.einsum("bsd,df->bsf", x, w3)
    h = _silu(gate) * up
    out = torch.einsum("bsf,fd->bsd", h, w2)
    return out
```

```cpp
#include <hip/hip_runtime.h>
#include <hip/hip_bf16.h>
#include <stdint.h>

constexpr int B = 8, D = 1024, F = 3584;
typedef unsigned v4u __attribute__((ext_vector_type(4)));

__device__ __forceinline__ float bf2f(unsigned short b) { return __builtin_bit_cast(float, (unsigned)b << 16); }
__device__ __forceinline__ float lo(unsigned u) { return __builtin_bit_cast(float, u << 16); }
__device__ __forceinline__ float hi(unsigned u) { return __builtin_bit_cast(float, u & 0xffff0000u); }
__device__ __forceinline__ float rbf(float f) { return __bfloat162float(__float2bfloat16(f)); }

__global__ void __launch_bounds__(256) k1(const unsigned short* x, const unsigned short* w1, const unsigned short* w3, unsigned short* h) {
    __shared__ float xs[B][D];
    __shared__ float red[4][2][B][16];
    const int tid = threadIdx.x;
    for (int i = tid; i < B * D; i += 256) xs[i / D][i % D] = bf2f(x[i]);
    __syncthreads();
    const int f0 = blockIdx.x * 16 + (tid & 1) * 8;
    const int r0 = tid >> 1;
    float g[B][8], u[B][8];
    for (int b = 0; b < B; ++b) for (int j = 0; j < 8; ++j) { g[b][j] = 0; u[b][j] = 0; }
    for (int k = r0; k < D; k += 128) {
        v4u a = *(const v4u*)(w1 + (size_t)k * F + f0);
        v4u c = *(const v4u*)(w3 + (size_t)k * F + f0);
        for (int b = 0; b < B; ++b) {
            float xv = xs[b][k];
            for (int j = 0; j < 4; ++j) {
                g[b][2*j] += xv * lo(a[j]); g[b][2*j+1] += xv * hi(a[j]);
                u[b][2*j] += xv * lo(c[j]); u[b][2*j+1] += xv * hi(c[j]);
            }
        }
    }
    for (int b = 0; b < B; ++b) for (int j = 0; j < 8; ++j) {
        for (int o = 2; o < 64; o <<= 1) { g[b][j] += __shfl_xor(g[b][j], o); u[b][j] += __shfl_xor(u[b][j], o); }
    }
    const int w = tid >> 6, lane = tid & 63;
    if (lane < 2) for (int b = 0; b < B; ++b) for (int j = 0; j < 8; ++j) { red[w][0][b][lane*8+j] = g[b][j]; red[w][1][b][lane*8+j] = u[b][j]; }
    __syncthreads();
    if (tid < 128) {
        int b = tid >> 4, j = tid & 15;
        float gg = red[0][0][b][j] + red[1][0][b][j] + red[2][0][b][j] + red[3][0][b][j];
        float uu = red[0][1][b][j] + red[1][1][b][j] + red[2][1][b][j] + red[3][1][b][j];
        gg = rbf(gg); uu = rbf(uu);
        float s = rbf(gg / (1.0f + __expf(-gg)));
        __hip_bfloat16 hv = __float2bfloat16(s * uu);
        h[b * F + blockIdx.x * 16 + j] = __builtin_bit_cast(unsigned short, hv);
    }
}

__global__ void __launch_bounds__(256) k2(const unsigned short* h, const unsigned short* w2, __hip_bfloat16* out) {
    __shared__ float red[4][B][8];
    const int tid = threadIdx.x;
    const int d0 = blockIdx.x * 8;
    float o[B][8];
    for (int b = 0; b < B; ++b) for (int j = 0; j < 8; ++j) o[b][j] = 0;
    for (int k = tid; k < F; k += 256) {
        v4u a = *(const v4u*)(w2 + (size_t)k * D + d0);
        for (int b = 0; b < B; ++b) {
            float hv = bf2f(h[b * F + k]);
            for (int j = 0; j < 4; ++j) { o[b][2*j] += hv * lo(a[j]); o[b][2*j+1] += hv * hi(a[j]); }
        }
    }
    for (int b = 0; b < B; ++b) for (int j = 0; j < 8; ++j)
        for (int s = 1; s < 64; s <<= 1) o[b][j] += __shfl_xor(o[b][j], s);
    const int w = tid >> 6, lane = tid & 63;
    if (lane == 0) for (int b = 0; b < B; ++b) for (int j = 0; j < 8; ++j) red[w][b][j] = o[b][j];
    __syncthreads();
    if (tid < 64) {
        int b = tid >> 3, j = tid & 7;
        out[b * D + d0 + j] = __float2bfloat16(red[0][b][j] + red[1][b][j] + red[2][b][j] + red[3][b][j]);
    }
}

extern "C" void kernel_launch(void* const* d_in, const int* in_sizes, int n_in,
                              void* d_out, int out_size, void* d_ws, size_t ws_size,
                              hipStream_t stream) {
    const unsigned short* x = (const unsigned short*)d_in[0];
    const unsigned short* w1 = (const unsigned short*)d_in[1];
    const unsigned short* w3 = (const unsigned short*)d_in[2];
    const unsigned short* w2 = (const unsigned short*)d_in[3];
    unsigned short* h = (unsigned short*)d_ws;
    k1<<<F / 16, 256, 0, stream>>>(x, w1, w3, h);
    k2<<<D / 8, 256, 0, stream>>>(h, w2, (__hip_bfloat16*)d_out);
}
```

```cpp
#include <hip/hip_runtime.h>
#include <hip/hip_bf16.h>
#include <stdint.h>

#define STAMPS 0

namespace {
constexpr int D = 1024, F = 3584;
constexpr int NWG = 224, NTHR = 768;
constexpr int FLAG_OFF = 4096;
constexpr int E1_OFF = 65536;
constexpr int E2_OFF = E1_OFF + NWG * 2048;
constexpr int WS_USED = E2_OFF + NWG * 4096;
constexpr int STAMP_OFF = 4 << 20;
constexpr int LDS_BYTES = 84 * 1024;
constexpr unsigned SPIN_CAP = 1u << 16;
constexpr int AUX_SC1 = 16;

#define GAS __attribute__((address_space(1)))
#define LAS __attribute__((address_space(3)))
typedef unsigned v4u __attribute__((ext_vector_type(4)));
typedef unsigned v2u __attribute__((ext_vector_type(2)));
typedef float f4 __attribute__((ext_vector_type(4)));
typedef short bf8 __attribute__((ext_vector_type(8)));
typedef __bf16 bf2 __attribute__((ext_vector_type(2)));

__device__ __forceinline__ f4 mfma(v4u a, v4u b, f4 c) {
    return __builtin_amdgcn_mfma_f32_16x16x32_bf16(__builtin_bit_cast(bf8, a), __builtin_bit_cast(bf8, b), c, 0, 0, 0);
}
__device__ __forceinline__ unsigned long long rt() {
    unsigned long long t;
    asm volatile("s_memrealtime %0\n\ts_waitcnt lgkmcnt(0)" : "=s"(t)::"memory");
    return t;
}
__device__ __forceinline__ v4u frag(const v4u (&w)[8], int e) {
    const unsigned sel = (e & 1) ? 0x07060302u : 0x05040100u;
    v4u f;
    f.x = __builtin_amdgcn_perm(w[1][e >> 1], w[0][e >> 1], sel);
    f.y = __builtin_amdgcn_perm(w[3][e >> 1], w[2][e >> 1], sel);
    f.z = __builtin_amdgcn_perm(w[5][e >> 1], w[4][e >> 1], sel);
    f.w = __builtin_amdgcn_perm(w[7][e >> 1], w[6][e >> 1], sel);
    return f;
}
__device__ __forceinline__ float rbf(float v) { return (float)(__bf16)v; }
__device__ __forceinline__ unsigned umax4(v4u v) { return max(max(v.x, v.y), max(v.z, v.w)); }
__device__ __forceinline__ v4u nosent(f4 v) {
    v4u u = __builtin_bit_cast(v4u, v);
    u.x = min(u.x, 0xFFFFFFFEu); u.y = min(u.y, 0xFFFFFFFEu); u.z = min(u.z, 0xFFFFFFFEu); u.w = min(u.w, 0xFFFFFFFEu);
    return u;
}
__device__ __forceinline__ float addswap32(float a) {
    unsigned u = __builtin_bit_cast(unsigned, a);
    auto r = __builtin_amdgcn_permlane32_swap(u, u, false, false);
    return __builtin_bit_cast(float, (unsigned)r[0]) + __builtin_bit_cast(float, (unsigned)r[1]);
}
__device__ __forceinline__ float addswap16(float a) {
    unsigned u = __builtin_bit_cast(unsigned, a);
    auto r = __builtin_amdgcn_permlane16_swap(u, u, false, false);
    return __builtin_bit_cast(float, (unsigned)r[0]) + __builtin_bit_cast(float, (unsigned)r[1]);
}

#define STAMP(i) do { } while (0)

__global__ void __launch_bounds__(NTHR) swiglu_fused(const unsigned char* __restrict__ x, const unsigned char* __restrict__ w1,
                                                     const unsigned char* __restrict__ w3, const unsigned char* __restrict__ w2,
                                                     unsigned short* __restrict__ out, unsigned char* ws) {
    extern __shared__ __attribute__((aligned(16))) unsigned char smem_[];
    LAS unsigned char* L = (LAS unsigned char*)smem_;
    STAMP(0);
    asm volatile("" ::"s"(x), "s"(w1), "s"(w3), "s"(w2), "s"(out), "s"(ws));
    const int lane = threadIdx.x & 63, wave = __builtin_amdgcn_readfirstlane(threadIdx.x >> 6);
    const int g = lane >> 4, c = lane & 15;
    const int bid = blockIdx.x;
    const int v = (bid & 7) * 28 + (bid >> 3);
    const int grp = v >> 3, mem = v & 7;
    const __amdgpu_buffer_rsrc_t rs = __builtin_amdgcn_make_buffer_rsrc(ws, 0, WS_USED, 0x00020000);
    LAS unsigned long long* ltag = (LAS unsigned long long*)(L + 49152);

    if (__builtin_expect(wave < 8, 1)) {
        const int strip = 2 * grp + (mem >> 2);
        const unsigned char* wm = (mem & 2) ? w3 : w1;
        const int k0 = (mem & 1) * 512 + wave * 64 + (c >> 3) * 32 + 8 * g;
        const v4u xf = *(const GAS v4u*)(x + ((c & 7) * D + k0) * 2);
        const unsigned char* p = wm + (size_t)k0 * (F * 2) + strip * 128 + (c & 7) * 16;
        v4u w[8];
#pragma unroll
        for (int j = 0; j < 8; ++j) w[j] = *(const GAS v4u*)(p + j * (F * 2));
        __builtin_amdgcn_sched_barrier(0);
        STAMP(1);
        __builtin_amdgcn_s_barrier();
        __builtin_amdgcn_sched_barrier(0);
        f4 acc[8];
#pragma unroll
        for (int e = 0; e < 8; ++e) acc[e] = mfma(frag(w, e), xf, f4{0.f, 0.f, 0.f, 0.f});
        STAMP(2);
        if ((g >> 1) == (c >> 3)) {
            LAS f4* dst = (LAS f4*)(L + (wave * 2 + (g >> 1)) * 2048 + ((g & 1) * 64 + (c & 7)) * 16);
#pragma unroll
            for (int e = 0; e < 8; ++e) dst[e * 8] = acc[e];
        }
        __syncthreads();
        {
            const LAS f4* src = (const LAS f4*)(L + g * 4 * 2048 + (wave * 16 + c) * 16);
            f4 s = (src[0] + src[128]) + (src[256] + src[384]);
#pragma unroll
            for (int i = 0; i < 4; ++i) s[i] = addswap16(addswap32(s[i]));
            if (lane < 16) __builtin_amdgcn_raw_buffer_store_b128(nosent(s), rs, E1_OFF + v * 2048 + (wave * 16 + c) * 16, 0, AUX_SC1);
        }
        STAMP(3);
        const unsigned long long tag = *ltag;
        if (wave == 0 && grp < 16) {
            const int fo = FLAG_OFF + ((lane < 28 ? lane : 27) * 8 + mem) * 128;
#pragma nounroll
            for (unsigned spins = 0; spins < SPIN_CAP; ++spins) {
                asm volatile("" ::: "memory");
                const v2u f = __builtin_amdgcn_raw_buffer_load_b64(rs, fo, 0, AUX_SC1);
                if (__all(f.x == (unsigned)tag && f.y == (unsigned)(tag >> 32))) break;
            }
        } else if (wave == 1 && v == 0) {
#pragma nounroll
            for (unsigned spins = 0; spins < SPIN_CAP; ++spins) {
                asm volatile("" ::: "memory");
                bool ok = true;
#pragma unroll
                for (int i = 0; i < 4; ++i) {
                    const int wgi = min(i * 64 + lane, NWG - 1);
                    const v2u f = __builtin_amdgcn_raw_buffer_load_b64(rs, FLAG_OFF + wgi * 128, 0, AUX_SC1);
                    ok &= f.x == (unsigned)tag && f.y == (unsigned)(tag >> 32);
                }
                if (__all(ok)) break;
            }
            if (lane == 0) __builtin_amdgcn_raw_buffer_store_b64(v2u{(unsigned)tag, (unsigned)(tag >> 32)}, rs, 0, 0, AUX_SC1);
        }
        STAMP(4);
        __builtin_amdgcn_s_barrier();
        if (wave == 0 && grp < 16) {
            const int hh = grp >> 3, cb = grp & 7;
            const int ro = E2_OFF + (g * 8 + mem) * 4096 + (hh * 128 + cb * 16 + c) * 16;
            v4u q[7];
            STAMP(5);
            for (unsigned spins = 0; spins < SPIN_CAP; ++spins) {
                asm volatile("" ::: "memory");
#pragma unroll
                for (int t = 0; t < 7; ++t) q[t] = __builtin_amdgcn_raw_buffer_load_b128(rs, ro + t * (32 * 4096), 0, AUX_SC1);
                unsigned mx = 0;
#pragma unroll
                for (int t = 0; t < 7; ++t) mx = max(mx, umax4(q[t]));
                if (__all(mx != 0xFFFFFFFFu)) break;
            }
            STAMP(6);
            f4 s = __builtin_bit_cast(f4, q[0]);
#pragma unroll
            for (int t = 1; t < 7; ++t) s += __builtin_bit_cast(f4, q[t]);
#pragma unroll
            for (int i = 0; i < 4; ++i) s[i] = addswap16(addswap32(s[i]));
            const float r = g == 0 ? s[0] : g == 1 ? s[1] : g == 2 ? s[2] : s[3];
            const __bf16 o = (__bf16)r;
            out[(hh * 4 + g) * D + mem * 128 + cb * 16 + c] = __builtin_bit_cast(unsigned short, o);
        }
    } else {
        const int u = wave - 8;
        if (u == 0) {
            const v2u ep = __builtin_amdgcn_raw_buffer_load_b64(rs, 0, 0, AUX_SC1);
            const v4u sent = {~0u, ~0u, ~0u, ~0u};
#pragma unroll
            for (int i = 0; i < 2; ++i) __builtin_amdgcn_raw_buffer_store_b128(sent, rs, E1_OFF + v * 2048 + i * 1024 + lane * 16, 0, AUX_SC1);
#pragma unroll
            for (int i = 0; i < 4; ++i) __builtin_amdgcn_raw_buffer_store_b128(sent, rs, E2_OFF + v * 4096 + i * 1024 + lane * 16, 0, AUX_SC1);
            asm volatile("s_waitcnt vmcnt(0)" ::: "memory");
            const unsigned long long tg = (((unsigned long long)ep.y << 32) | ep.x) + 1ull;
            if (lane == 0) {
                __builtin_amdgcn_raw_buffer_store_b64(v2u{(unsigned)tg, (unsigned)(tg >> 32)}, rs, FLAG_OFF + v * 128, 0, AUX_SC1);
                *ltag = tg;
            }
            asm volatile("s_waitcnt lgkmcnt(0)" ::: "memory");
        }
        STAMP(1);
        __builtin_amdgcn_s_barrier();
        asm volatile("" ::: "memory");
        const int pv = grp * 8 + (u >> 1) * 4;
        const int fo = FLAG_OFF + (pv + (lane & 3)) * 128;
        v2u fl = __builtin_amdgcn_raw_buffer_load_b64(rs, fo, 0, AUX_SC1);
        const unsigned char* p = w2 + (size_t)(grp * 128 + u * 32 + 2 * g) * (D * 2) + mem * 256 + c * 16;
        v4u w[8];
#pragma unroll
        for (int j = 0; j < 8; ++j) w[j] = *(const GAS v4u*)(p + (8 * (j & 3) + (j >> 2)) * (D * 2));
        __builtin_amdgcn_sched_barrier(0);
        STAMP(2);
        __builtin_amdgcn_s_barrier();
        __builtin_amdgcn_sched_barrier(0);
        const unsigned long long tag = *ltag;
#pragma nounroll
        for (unsigned spins = 0; spins < SPIN_CAP; ++spins) {
            if (__all(fl.x == (unsigned)tag && fl.y == (unsigned)(tag >> 32))) break;
            asm volatile("" ::: "memory");
            fl = __builtin_amdgcn_raw_buffer_load_b64(rs, fo, 0, AUX_SC1);
        }
        v4u fr[8];
#pragma unroll
        for (int e = 0; e < 8; ++e) { fr[e] = frag(w, e); asm volatile("" : "+v"(fr[e])); }
        STAMP(3);
        const int eo = E1_OFF + pv * 2048 + (u & 1) * 1024 + lane * 16;
        v4u q[4];
        for (unsigned spins = 0; spins < SPIN_CAP; ++spins) {
            asm volatile("" ::: "memory");
#pragma unroll
            for (int t = 0; t < 4; ++t) q[t] = __builtin_amdgcn_raw_buffer_load_b128(rs, eo + t * 2048, 0, AUX_SC1);
            const unsigned mx = max(max(umax4(q[0]), umax4(q[1])), max(umax4(q[2]), umax4(q[3])));
            if (__all(mx != 0xFFFFFFFFu)) break;
        }
        STAMP(4);
        const f4 gs = __builtin_bit_cast(f4, q[0]) + __builtin_bit_cast(f4, q[1]);
        const f4 us = __builtin_bit_cast(f4, q[2]) + __builtin_bit_cast(f4, q[3]);
        float hv[4];
#pragma unroll
        for (int i = 0; i < 4; ++i) {
            const float gg = rbf(gs[i]), uu = rbf(us[i]);
            const float sl = rbf(gg * __builtin_amdgcn_rcpf(1.0f + __expf(-gg)));
            hv[i] = sl * uu;
        }
        const bf2 h01 = {(__bf16)hv[0], (__bf16)hv[1]}, h23 = {(__bf16)hv[2], (__bf16)hv[3]};
        v4u a;
        a.x = __builtin_bit_cast(unsigned, h01);
        a.y = __builtin_bit_cast(unsigned, h23);
        a.z = __builtin_amdgcn_update_dpp(0u, a.x, 0x128, 0xf, 0xf, false);
        a.w = __builtin_amdgcn_update_dpp(0u, a.y, 0x128, 0xf, 0xf, false);
        f4 acc[8];
#pragma unroll
        for (int e = 0; e < 8; ++e) acc[e] = mfma(a, fr[e], f4{0.f, 0.f, 0.f, 0.f});
        if (lane < 32) {
            LAS unsigned char* dst = L + 32768 + u * 4096 + (g * 128 + c * 8) * 16;
#pragma unroll
            for (int e = 0; e < 8; ++e) *(LAS f4*)(dst + ((e ^ (c & 7)) * 16)) = acc[e];
        }
        asm volatile("s_waitcnt lgkmcnt(0)" ::: "memory");
        STAMP(5);
        __builtin_amdgcn_s_barrier();
        asm volatile("" ::: "memory");
        {
            const int t = u * 64 + lane;
            const LAS f4* src = (const LAS f4*)(L + 32768 + ((t & ~7) + ((t & 7) ^ ((t >> 3) & 7))) * 16);
            const f4 s = (src[0] + src[256]) + (src[512] + src[768]);
            __builtin_amdgcn_raw_buffer_store_b128(nosent(s), rs, E2_OFF + v * 4096 + t * 16, 0, AUX_SC1);
        }
        STAMP(6);
    }
}
}

extern "C" void kernel_launch(void* const* d_in, const int* in_sizes, int n_in, void* d_out, int out_size, void* d_ws, size_t ws_size,
                              hipStream_t stream) {
    swiglu_fused<<<NWG, NTHR, LDS_BYTES, stream>>>((const unsigned char*)d_in[0], (const unsigned char*)d_in[1], (const unsigned char*)d_in[2],
                                                   (const unsigned char*)d_in[3], (unsigned short*)d_out, (unsigned char*)d_ws);
}
```

```cpp
#define STAMPS 1
#define PROBE_BID 8
#define PROBE_WAVE 0
#define PROBE_FROM 0
#define PROBE_TO 0
#include <hip/hip_runtime.h>
#include <hip/hip_bf16.h>
#include <stdint.h>

namespace {
constexpr int D = 1024, F = 3584;
constexpr int NWG = 224, NTHR = 768;
constexpr int FLAG_OFF = 4096;
constexpr int E1_OFF = 65536;
constexpr int E2_OFF = E1_OFF + NWG * 2048;
constexpr int WS_USED = E2_OFF + NWG * 4096;
constexpr int STAMP_OFF = 4 << 20;
constexpr int LDS_BYTES = 84 * 1024;
constexpr unsigned SPIN_CAP = 1u << 16;
constexpr int AUX_SC1 = 16;

#define GAS __attribute__((address_space(1)))
#define LAS __attribute__((address_space(3)))
typedef unsigned v4u __attribute__((ext_vector_type(4)));
typedef unsigned v2u __attribute__((ext_vector_type(2)));
typedef float f4 __attribute__((ext_vector_type(4)));
typedef short bf8 __attribute__((ext_vector_type(8)));
typedef __bf16 bf2 __attribute__((ext_vector_type(2)));

__device__ __forceinline__ f4 mfma(v4u a, v4u b, f4 c) {
    return __builtin_amdgcn_mfma_f32_16x16x32_bf16(__builtin_bit_cast(bf8, a), __builtin_bit_cast(bf8, b), c, 0, 0, 0);
}
__device__ __forceinline__ unsigned long long rt() {
    unsigned long long t;
    asm volatile("s_memrealtime %0\n\ts_waitcnt lgkmcnt(0)" : "=s"(t)::"memory");
    return t;
}
__device__ __forceinline__ v4u frag(const v4u (&w)[8], int e) {
    const unsigned sel = (e & 1) ? 0x07060302u : 0x05040100u;
    v4u f;
    f.x = __builtin_amdgcn_perm(w[1][e >> 1], w[0][e >> 1], sel);
    f.y = __builtin_amdgcn_perm(w[3][e >> 1], w[2][e >> 1], sel);
    f.z = __builtin_amdgcn_perm(w[5][e >> 1], w[4][e >> 1], sel);
    f.w = __builtin_amdgcn_perm(w[7][e >> 1], w[6][e >> 1], sel);
    return f;
}
__device__ __forceinline__ float rbf(float v) { return (float)(__bf16)v; }
__device__ __forceinline__ unsigned umax4(v4u v) { return max(max(v.x, v.y), max(v.z, v.w)); }
__device__ __forceinline__ v4u nosent(f4 v) {
    v4u u = __builtin_bit_cast(v4u, v);
    u.x = min(u.x, 0xFFFFFFFEu); u.y = min(u.y, 0xFFFFFFFEu); u.z = min(u.z, 0xFFFFFFFEu); u.w = min(u.w, 0xFFFFFFFEu);
    return u;
}
__device__ __forceinline__ float addswap32(float a) {
    unsigned u = __builtin_bit_cast(unsigned, a);
    auto r = __builtin_amdgcn_permlane32_swap(u, u, false, false);
    return __builtin_bit_cast(float, (unsigned)r[0]) + __builtin_bit_cast(float, (unsigned)r[1]);
}
__device__ __forceinline__ float addswap16(float a) {
    unsigned u = __builtin_bit_cast(unsigned, a);
    auto r = __builtin_amdgcn_permlane16_swap(u, u, false, false);
    return __builtin_bit_cast(float, (unsigned)r[0]) + __builtin_bit_cast(float, (unsigned)r[1]);
}

#define STAMP(i) do { __builtin_amdgcn_sched_barrier(0); st[i] = rt(); __builtin_amdgcn_sched_barrier(0); } while (0)

__global__ void __launch_bounds__(NTHR) swiglu_fused(const unsigned char* __restrict__ x, const unsigned char* __restrict__ w1,
                                                     const unsigned char* __restrict__ w3, const unsigned char* __restrict__ w2,
                                                     unsigned short* __restrict__ out, unsigned char* ws) {
    extern __shared__ __attribute__((aligned(16))) unsigned char smem_[];
    LAS unsigned char* L = (LAS unsigned char*)smem_;
    unsigned long long st[8] = {0, 0, 0, 0, 0, 0, 0, 0};
    STAMP(0);
    asm volatile("" ::"s"(x), "s"(w1), "s"(w3), "s"(w2), "s"(out), "s"(ws));
    const int lane = threadIdx.x & 63, wave = __builtin_amdgcn_readfirstlane(threadIdx.x >> 6);
    const int g = lane >> 4, c = lane & 15;
    const int bid = blockIdx.x;
    const int v = (bid & 7) * 28 + (bid >> 3);
    const int grp = v >> 3, mem = v & 7;
    const __amdgpu_buffer_rsrc_t rs = __builtin_amdgcn_make_buffer_rsrc(ws, 0, WS_USED, 0x00020000);
    LAS unsigned long long* ltag = (LAS unsigned long long*)(L + 49152);

    if (__builtin_expect(wave < 8, 1)) {
        const int strip = 2 * grp + (mem >> 2);
        const unsigned char* wm = (mem & 2) ? w3 : w1;
        const int k0 = (mem & 1) * 512 + wave * 64 + (c >> 3) * 32 + 8 * g;
        const v4u xf = *(const GAS v4u*)(x + ((c & 7) * D + k0) * 2);
        const unsigned char* p = wm + (size_t)k0 * (F * 2) + strip * 128 + (c & 7) * 16;
        v4u w[8];
#pragma unroll
        for (int j = 0; j < 8; ++j) w[j] = *(const GAS v4u*)(p + j * (F * 2));
        __builtin_amdgcn_sched_barrier(0);
        STAMP(1);
        __builtin_amdgcn_s_barrier();
        __builtin_amdgcn_sched_barrier(0);
        f4 acc[8];
#pragma unroll
        for (int e = 0; e < 8; ++e) acc[e] = mfma(frag(w, e), xf, f4{0.f, 0.f, 0.f, 0.f});
        STAMP(2);
        if ((g >> 1) == (c >> 3)) {
            LAS f4* dst = (LAS f4*)(L + (wave * 2 + (g >> 1)) * 2048 + ((g & 1) * 64 + (c & 7)) * 16);
#pragma unroll
            for (int e = 0; e < 8; ++e) dst[e * 8] = acc[e];
        }
        __syncthreads();
        {
            const LAS f4* src = (const LAS f4*)(L + g * 4 * 2048 + (wave * 16 + c) * 16);
            f4 s = (src[0] + src[128]) + (src[256] + src[384]);
#pragma unroll
            for (int i = 0; i < 4; ++i) s[i] = addswap16(addswap32(s[i]));
            if (lane < 16) __builtin_amdgcn_raw_buffer_store_b128(nosent(s), rs, E1_OFF + v * 2048 + (wave * 16 + c) * 16, 0, AUX_SC1);
        }
        STAMP(3);
        const unsigned long long tag = *ltag;
        if (wave == 0 && grp < 16) {
            const int fo = FLAG_OFF + ((lane < 28 ? lane : 27) * 8 + mem) * 128;
#pragma nounroll
            for (unsigned spins = 0; spins < SPIN_CAP; ++spins) {
                asm volatile("" ::: "memory");
                const v2u f = __builtin_amdgcn_raw_buffer_load_b64(rs, fo, 0, AUX_SC1);
                if (__all(f.x == (unsigned)tag && f.y == (unsigned)(tag >> 32))) break;
            }
        } else if (wave == 1 && v == 0) {
#pragma nounroll
            for (unsigned spins = 0; spins < SPIN_CAP; ++spins) {
                asm volatile("" ::: "memory");
                bool ok = true;
#pragma unroll
                for (int i = 0; i < 4; ++i) {
                    const int wgi = min(i * 64 + lane, NWG - 1);
                    const v2u f = __builtin_amdgcn_raw_buffer_load_b64(rs, FLAG_OFF + wgi * 128, 0, AUX_SC1);
                    ok &= f.x == (unsigned)tag && f.y == (unsigned)(tag >> 32);
                }
                if (__all(ok)) break;
            }
            if (lane == 0) __builtin_amdgcn_raw_buffer_store_b64(v2u{(unsigned)tag, (unsigned)(tag >> 32)}, rs, 0, 0, AUX_SC1);
        }
        STAMP(4);
        __builtin_amdgcn_s_barrier();
        if (wave == 0 && grp < 16) {
            const int hh = grp >> 3, cb = grp & 7;
            const int ro = E2_OFF + (g * 8 + mem) * 4096 + (hh * 128 + cb * 16 + c) * 16;
            v4u q[7];
            STAMP(5);
            for (unsigned spins = 0; spins < SPIN_CAP; ++spins) {
                asm volatile("" ::: "memory");
#pragma unroll
                for (int t = 0; t < 7; ++t) q[t] = __builtin_amdgcn_raw_buffer_load_b128(rs, ro + t * (32 * 4096), 0, AUX_SC1);
                unsigned mx = 0;
#pragma unroll
                for (int t = 0; t < 7; ++t) mx = max(mx, umax4(q[t]));
                if (__all(mx != 0xFFFFFFFFu)) break;
            }
            STAMP(6);
            f4 s = __builtin_bit_cast(f4, q[0]);
#pragma unroll
            for (int t = 1; t < 7; ++t) s += __builtin_bit_cast(f4, q[t]);
#pragma unroll
            for (int i = 0; i < 4; ++i) s[i] = addswap16(addswap32(s[i]));
            const float r = g == 0 ? s[0] : g == 1 ? s[1] : g == 2 ? s[2] : s[3];
            const __bf16 o = (__bf16)r;
            out[(hh * 4 + g) * D + mem * 128 + cb * 16 + c] = __builtin_bit_cast(unsigned short, o);
        }
    } else {
        const int u = wave - 8;
        if (u == 0) {
            const v2u ep = __builtin_amdgcn_raw_buffer_load_b64(rs, 0, 0, AUX_SC1);
            const v4u sent = {~0u, ~0u, ~0u, ~0u};
#pragma unroll
            for (int i = 0; i < 2; ++i) __builtin_amdgcn_raw_buffer_store_b128(sent, rs, E1_OFF + v * 2048 + i * 1024 + lane * 16, 0, AUX_SC1);
#pragma unroll
            for (int i = 0; i < 4; ++i) __builtin_amdgcn_raw_buffer_store_b128(sent, rs, E2_OFF + v * 4096 + i * 1024 + lane * 16, 0, AUX_SC1);
            asm volatile("s_waitcnt vmcnt(0)" ::: "memory");
            const unsigned long long tg = (((unsigned long long)ep.y << 32) | ep.x) + 1ull;
            if (lane == 0) {
                __builtin_amdgcn_raw_buffer_store_b64(v2u{(unsigned)tg, (unsigned)(tg >> 32)}, rs, FLAG_OFF + v * 128, 0, AUX_SC1);
                *ltag = tg;
            }
            asm volatile("s_waitcnt lgkmcnt(0)" ::: "memory");
        }
        STAMP(1);
        __builtin_amdgcn_s_barrier();
        asm volatile("" ::: "memory");
        const int pv = grp * 8 + (u >> 1) * 4;
        const int fo = FLAG_OFF + (pv + (lane & 3)) * 128;
        v2u fl = __builtin_amdgcn_raw_buffer_load_b64(rs, fo, 0, AUX_SC1);
        const unsigned char* p = w2 + (size_t)(grp * 128 + u * 32 + 2 * g) * (D * 2) + mem * 256 + c * 16;
        v4u w[8];
#pragma unroll
        for (int j = 0; j < 8; ++j) w[j] = *(const GAS v4u*)(p + (8 * (j & 3) + (j >> 2)) * (D * 2));
        __builtin_amdgcn_sched_barrier(0);
        STAMP(2);
        __builtin_amdgcn_s_barrier();
        __builtin_amdgcn_sched_barrier(0);
        const unsigned long long tag = *ltag;
#pragma nounroll
        for (unsigned spins = 0; spins < SPIN_CAP; ++spins) {
            if (__all(fl.x == (unsigned)tag && fl.y == (unsigned)(tag >> 32))) break;
            asm volatile("" ::: "memory");
            fl = __builtin_amdgcn_raw_buffer_load_b64(rs, fo, 0, AUX_SC1);
        }
        v4u fr[8];
#pragma unroll
        for (int e = 0; e < 8; ++e) { fr[e] = frag(w, e); asm volatile("" : "+v"(fr[e])); }
        STAMP(3);
        const int eo = E1_OFF + pv * 2048 + (u & 1) * 1024 + lane * 16;
        v4u q[4];
        for (unsigned spins = 0; spins < SPIN_CAP; ++spins) {
            asm volatile("" ::: "memory");
#pragma unroll
            for (int t = 0; t < 4; ++t) q[t] = __builtin_amdgcn_raw_buffer_load_b128(rs, eo + t * 2048, 0, AUX_SC1);
            const unsigned mx = max(max(umax4(q[0]), umax4(q[1])), max(umax4(q[2]), umax4(q[3])));
            if (__all(mx != 0xFFFFFFFFu)) break;
        }
        STAMP(4);
        const f4 gs = __builtin_bit_cast(f4, q[0]) + __builtin_bit_cast(f4, q[1]);
        const f4 us = __builtin_bit_cast(f4, q[2]) + __builtin_bit_cast(f4, q[3]);
        float hv[4];
#pragma unroll
        for (int i = 0; i < 4; ++i) {
            const float gg = rbf(gs[i]), uu = rbf(us[i]);
            const float sl = rbf(gg * __builtin_amdgcn_rcpf(1.0f + __expf(-gg)));
            hv[i] = sl * uu;
        }
        const bf2 h01 = {(__bf16)hv[0], (__bf16)hv[1]}, h23 = {(__bf16)hv[2], (__bf16)hv[3]};
        v4u a;
        a.x = __builtin_bit_cast(unsigned, h01);
        a.y = __builtin_bit_cast(unsigned, h23);
        a.z = __builtin_amdgcn_update_dpp(0u, a.x, 0x128, 0xf, 0xf, false);
        a.w = __builtin_amdgcn_update_dpp(0u, a.y, 0x128, 0xf, 0xf, false);
        f4 acc[8];
#pragma unroll
        for (int e = 0; e < 8; ++e) acc[e] = mfma(a, fr[e], f4{0.f, 0.f, 0.f, 0.f});
        if (lane < 32) {
            LAS unsigned char* dst = L + 32768 + u * 4096 + (g * 128 + c * 8) * 16;
#pragma unroll
            for (int e = 0; e < 8; ++e) *(LAS f4*)(dst + ((e ^ (c & 7)) * 16)) = acc[e];
        }
        asm volatile("s_waitcnt lgkmcnt(0)" ::: "memory");
        STAMP(5);
        __builtin_amdgcn_s_barrier();
        asm volatile("" ::: "memory");
        {
            const int t = u * 64 + lane;
            const LAS f4* src = (const LAS f4*)(L + 32768 + ((t & ~7) + ((t & 7) ^ ((t >> 3) & 7))) * 16);
            const f4 s = (src[0] + src[256]) + (src[512] + src[768]);
            __builtin_amdgcn_raw_buffer_store_b128(nosent(s), rs, E2_OFF + v * 4096 + t * 16, 0, AUX_SC1);
        }
        STAMP(6);
    }
    STAMP(7);
    if (bid == PROBE_BID && wave == PROBE_WAVE) {
        const unsigned long long until = st[0] + 3000 + 10 * (st[PROBE_TO] - st[PROBE_FROM]);
        while (rt() < until) {}
    }
}
}

extern "C" void kernel_launch(void* const* d_in, const int* in_sizes, int n_in, void* d_out, int out_size, void* d_ws, size_t ws_size,
                              hipStream_t stream) {
    swiglu_fused<<<NWG, NTHR, LDS_BYTES, stream>>>((const unsigned char*)d_in[0], (const unsigned char*)d_in[1], (const unsigned char*)d_in[2],
                                                   (const unsigned char*)d_in[3], (unsigned short*)d_out, (unsigned char*)d_ws);
}
```

```cpp
#define STAMPS 1
#define PROBE_COND (bid == 8 && wave == 0)
#define PROBE_EXTRA (0)
#define LDSB (84*1024)
#include <hip/hip_runtime.h>
#include <hip/hip_bf16.h>
#include <stdint.h>

namespace {
constexpr int D = 1024, F = 3584;
constexpr int NWG = 224, NTHR = 768;
constexpr int FLAG_OFF = 4096;
constexpr int E1_OFF = 65536;
constexpr int E2_OFF = E1_OFF + NWG * 2048;
constexpr int WS_USED = E2_OFF + NWG * 4096;
constexpr int STAMP_OFF = 4 << 20;
constexpr int LDS_BYTES = LDSB;
constexpr unsigned SPIN_CAP = 1u << 16;
constexpr int AUX_SC1 = 16;

#define GAS __attribute__((address_space(1)))
#define LAS __attribute__((address_space(3)))
typedef unsigned v4u __attribute__((ext_vector_type(4)));
typedef unsigned v2u __attribute__((ext_vector_type(2)));
typedef float f4 __attribute__((ext_vector_type(4)));
typedef short bf8 __attribute__((ext_vector_type(8)));
typedef __bf16 bf2 __attribute__((ext_vector_type(2)));

__device__ __forceinline__ f4 mfma(v4u a, v4u b, f4 c) {
    return __builtin_amdgcn_mfma_f32_16x16x32_bf16(__builtin_bit_cast(bf8, a), __builtin_bit_cast(bf8, b), c, 0, 0, 0);
}
__device__ __forceinline__ unsigned long long rt() {
    unsigned long long t;
    asm volatile("s_memrealtime %0\n\ts_waitcnt lgkmcnt(0)" : "=s"(t)::"memory");
    return t;
}
__device__ __forceinline__ v4u frag(const v4u (&w)[8], int e) {
    const unsigned sel = (e & 1) ? 0x07060302u : 0x05040100u;
    v4u f;
    f.x = __builtin_amdgcn_perm(w[1][e >> 1], w[0][e >> 1], sel);
    f.y = __builtin_amdgcn_perm(w[3][e >> 1], w[2][e >> 1], sel);
    f.z = __builtin_amdgcn_perm(w[5][e >> 1], w[4][e >> 1], sel);
    f.w = __builtin_amdgcn_perm(w[7][e >> 1], w[6][e >> 1], sel);
    return f;
}
__device__ __forceinline__ float rbf(float v) { return (float)(__bf16)v; }
__device__ __forceinline__ unsigned umax4(v4u v) { return max(max(v.x, v.y), max(v.z, v.w)); }
__device__ __forceinline__ v4u nosent(f4 v) {
    v4u u = __builtin_bit_cast(v4u, v);
    u.x = min(u.x, 0xFFFFFFFEu); u.y = min(u.y, 0xFFFFFFFEu); u.z = min(u.z, 0xFFFFFFFEu); u.w = min(u.w, 0xFFFFFFFEu);
    return u;
}
__device__ __forceinline__ float addswap32(float a) {
    unsigned u = __builtin_bit_cast(unsigned, a);
    auto r = __builtin_amdgcn_permlane32_swap(u, u, false, false);
    return __builtin_bit_cast(float, (unsigned)r[0]) + __builtin_bit_cast(float, (unsigned)r[1]);
}
__device__ __forceinline__ float addswap16(float a) {
    unsigned u = __builtin_bit_cast(unsigned, a);
    auto r = __builtin_amdgcn_permlane16_swap(u, u, false, false);
    return __builtin_bit_cast(float, (unsigned)r[0]) + __builtin_bit_cast(float, (unsigned)r[1]);
}

#define STAMP(i) do { __builtin_amdgcn_sched_barrier(0); st[i] = rt(); __builtin_amdgcn_sched_barrier(0); } while (0)

struct KA { const unsigned char *x, *w1, *w3, *w2; unsigned short* out; unsigned char* ws; };
__global__ void __launch_bounds__(NTHR) swiglu_fused(const unsigned char* __restrict__ x_, const unsigned char* __restrict__ w1_,
                                                     const unsigned char* __restrict__ w3_, const unsigned char* __restrict__ w2_,
                                                     unsigned short* __restrict__ out_, unsigned char* ws_) {
    const unsigned long long tentry = rt();
    __builtin_amdgcn_sched_barrier(0);
    const __attribute__((address_space(4))) KA* ka = (const __attribute__((address_space(4))) KA*)__builtin_amdgcn_kernarg_segment_ptr();
    asm volatile("" : "+s"(ka));
    const unsigned char *x = ka->x, *w1 = ka->w1, *w3 = ka->w3, *w2 = ka->w2; unsigned short* out = ka->out; unsigned char* ws = ka->ws;
    asm volatile("" ::"s"(x), "s"(w1), "s"(w3), "s"(w2), "s"(out), "s"(ws));
    extern __shared__ __attribute__((aligned(16))) unsigned char smem_[];
    LAS unsigned char* L = (LAS unsigned char*)smem_;
    unsigned long long st[8] = {0, 0, 0, 0, 0, 0, 0, 0};
    STAMP(0);
    asm volatile("" ::"s"(x), "s"(w1), "s"(w3), "s"(w2), "s"(out), "s"(ws));
    const int lane = threadIdx.x & 63, wave = __builtin_amdgcn_readfirstlane(threadIdx.x >> 6);
    const int g = lane >> 4, c = lane & 15;
    const int bid = blockIdx.x;
    const int v = (bid & 7) * 28 + (bid >> 3);
    const int grp = v >> 3, mem = v & 7;
    const __amdgpu_buffer_rsrc_t rs = __builtin_amdgcn_make_buffer_rsrc(ws, 0, WS_USED, 0x00020000);
    LAS unsigned long long* ltag = (LAS unsigned long long*)(L + 49152);

    if (__builtin_expect(wave < 8, 1)) {
        const int strip = 2 * grp + (mem >> 2);
        const unsigned char* wm = (mem & 2) ? w3 : w1;
        const int k0 = (mem & 1) * 512 + wave * 64 + (c >> 3) * 32 + 8 * g;
        const v4u xf = *(const GAS v4u*)(x + ((c & 7) * D + k0) * 2);
        const unsigned char* p = wm + (size_t)k0 * (F * 2) + strip * 128 + (c & 7) * 16;
        v4u w[8];
#pragma unroll
        for (int j = 0; j < 8; ++j) w[j] = *(const GAS v4u*)(p + j * (F * 2));
        __builtin_amdgcn_sched_barrier(0);
        STAMP(1);
        __builtin_amdgcn_s_barrier();
        __builtin_amdgcn_sched_barrier(0);
        f4 acc[8];
#pragma unroll
        for (int e = 0; e < 8; ++e) acc[e] = mfma(frag(w, e), xf, f4{0.f, 0.f, 0.f, 0.f});
        STAMP(2);
        if ((g >> 1) == (c >> 3)) {
            LAS f4* dst = (LAS f4*)(L + (wave * 2 + (g >> 1)) * 2048 + ((g & 1) * 64 + (c & 7)) * 16);
#pragma unroll
            for (int e = 0; e < 8; ++e) dst[e * 8] = acc[e];
        }
        __syncthreads();
        {
            const LAS f4* src = (const LAS f4*)(L + g * 4 * 2048 + (wave * 16 + c) * 16);
            f4 s = (src[0] + src[128]) + (src[256] + src[384]);
#pragma unroll
            for (int i = 0; i < 4; ++i) s[i] = addswap16(addswap32(s[i]));
            if (lane < 16) __builtin_amdgcn_raw_buffer_store_b128(nosent(s), rs, E1_OFF + v * 2048 + (wave * 16 + c) * 16, 0, AUX_SC1);
        }
        STAMP(3);
        const unsigned long long tag = *ltag;
        if (wave == 0 && grp < 16) {
            const int fo = FLAG_OFF + ((lane < 28 ? lane : 27) * 8 + mem) * 128;
#pragma nounroll
            for (unsigned spins = 0; spins < SPIN_CAP; ++spins) {
                asm volatile("" ::: "memory");
                const v2u f = __builtin_amdgcn_raw_buffer_load_b64(rs, fo, 0, AUX_SC1);
                if (__all(f.x == (unsigned)tag && f.y == (unsigned)(tag >> 32))) break;
            }
        } else if (wave == 1 && v == 0) {
#pragma nounroll
            for (unsigned spins = 0; spins < SPIN_CAP; ++spins) {
                asm volatile("" ::: "memory");
                bool ok = true;
#pragma unroll
                for (int i = 0; i < 4; ++i) {
                    const int wgi = min(i * 64 + lane, NWG - 1);
                    const v2u f = __builtin_amdgcn_raw_buffer_load_b64(rs, FLAG_OFF + wgi * 128, 0, AUX_SC1);
                    ok &= f.x == (unsigned)tag && f.y == (unsigned)(tag >> 32);
                }
                if (__all(ok)) break;
            }
            if (lane == 0) __builtin_amdgcn_raw_buffer_store_b64(v2u{(unsigned)tag, (unsigned)(tag >> 32)}, rs, 0, 0, AUX_SC1);
        }
        STAMP(4);
        __builtin_amdgcn_s_barrier();
        if (wave == 0 && grp < 16) {
            const int hh = grp >> 3, cb = grp & 7;
            const int ro = E2_OFF + (g * 8 + mem) * 4096 + (hh * 128 + cb * 16 + c) * 16;
            v4u q[7];
            STAMP(5);
            for (unsigned spins = 0; spins < SPIN_CAP; ++spins) {
                asm volatile("" ::: "memory");
#pragma unroll
                for (int t = 0; t < 7; ++t) q[t] = __builtin_amdgcn_raw_buffer_load_b128(rs, ro + t * (32 * 4096), 0, AUX_SC1);
                unsigned mx = 0;
#pragma unroll
                for (int t = 0; t < 7; ++t) mx = max(mx, umax4(q[t]));
                if (__all(mx != 0xFFFFFFFFu)) break;
            }
            STAMP(6);
            f4 s = __builtin_bit_cast(f4, q[0]);
#pragma unroll
            for (int t = 1; t < 7; ++t) s += __builtin_bit_cast(f4, q[t]);
#pragma unroll
            for (int i = 0; i < 4; ++i) s[i] = addswap16(addswap32(s[i]));
            const float r = g == 0 ? s[0] : g == 1 ? s[1] : g == 2 ? s[2] : s[3];
            const __bf16 o = (__bf16)r;
            out[(hh * 4 + g) * D + mem * 128 + cb * 16 + c] = __builtin_bit_cast(unsigned short, o);
        }
    } else {
        const int u = wave - 8;
        if (u == 0) {
            const v2u ep = __builtin_amdgcn_raw_buffer_load_b64(rs, 0, 0, AUX_SC1);
            const v4u sent = {~0u, ~0u, ~0u, ~0u};
#pragma unroll
            for (int i = 0; i < 2; ++i) __builtin_amdgcn_raw_buffer_store_b128(sent, rs, E1_OFF + v * 2048 + i * 1024 + lane * 16, 0, AUX_SC1);
#pragma unroll
            for (int i = 0; i < 4; ++i) __builtin_amdgcn_raw_buffer_store_b128(sent, rs, E2_OFF + v * 4096 + i * 1024 + lane * 16, 0, AUX_SC1);
            asm volatile("s_waitcnt vmcnt(0)" ::: "memory");
            const unsigned long long tg = (((unsigned long long)ep.y << 32) | ep.x) + 1ull;
            if (lane == 0) {
                __builtin_amdgcn_raw_buffer_store_b64(v2u{(unsigned)tg, (unsigned)(tg >> 32)}, rs, FLAG_OFF + v * 128, 0, AUX_SC1);
                *ltag = tg;
            }
            asm volatile("s_waitcnt lgkmcnt(0)" ::: "memory");
        }
        STAMP(1);
        __builtin_amdgcn_s_barrier();
        asm volatile("" ::: "memory");
        const int pv = grp * 8 + (u >> 1) * 4;
        const int fo = FLAG_OFF + (pv + (lane & 3)) * 128;
        v2u fl = __builtin_amdgcn_raw_buffer_load_b64(rs, fo, 0, AUX_SC1);
        const unsigned char* p = w2 + (size_t)(grp * 128 + u * 32 + 2 * g) * (D * 2) + mem * 256 + c * 16;
        v4u w[8];
#pragma unroll
        for (int j = 0; j < 8; ++j) w[j] = *(const GAS v4u*)(p + (8 * (j & 3) + (j >> 2)) * (D * 2));
        __builtin_amdgcn_sched_barrier(0);
        STAMP(2);
        __builtin_amdgcn_s_barrier();
        __builtin_amdgcn_sched_barrier(0);
        const unsigned long long tag = *ltag;
#pragma nounroll
        for (unsigned spins = 0; spins < SPIN_CAP; ++spins) {
            if (__all(fl.x == (unsigned)tag && fl.y == (unsigned)(tag >> 32))) break;
            asm volatile("" ::: "memory");
            fl = __builtin_amdgcn_raw_buffer_load_b64(rs, fo, 0, AUX_SC1);
        }
        v4u fr[8];
#pragma unroll
        for (int e = 0; e < 8; ++e) { fr[e] = frag(w, e); asm volatile("" : "+v"(fr[e])); }
        STAMP(3);
        const int eo = E1_OFF + pv * 2048 + (u & 1) * 1024 + lane * 16;
        v4u q[4];
        for (unsigned spins = 0; spins < SPIN_CAP; ++spins) {
            asm volatile("" ::: "memory");
#pragma unroll
            for (int t = 0; t < 4; ++t) q[t] = __builtin_amdgcn_raw_buffer_load_b128(rs, eo + t * 2048, 0, AUX_SC1);
            const unsigned mx = max(max(umax4(q[0]), umax4(q[1])), max(umax4(q[2]), umax4(q[3])));
            if (__all(mx != 0xFFFFFFFFu)) break;
        }
        STAMP(4);
        const f4 gs = __builtin_bit_cast(f4, q[0]) + __builtin_bit_cast(f4, q[1]);
        const f4 us = __builtin_bit_cast(f4, q[2]) + __builtin_bit_cast(f4, q[3]);
        float hv[4];
#pragma unroll
        for (int i = 0; i < 4; ++i) {
            const float gg = rbf(gs[i]), uu = rbf(us[i]);
            const float sl = rbf(gg * __builtin_amdgcn_rcpf(1.0f + __expf(-gg)));
            hv[i] = sl * uu;
        }
        const bf2 h01 = {(__bf16)hv[0], (__bf16)hv[1]}, h23 = {(__bf16)hv[2], (__bf16)hv[3]};
        v4u a;
        a.x = __builtin_bit_cast(unsigned, h01);
        a.y = __builtin_bit_cast(unsigned, h23);
        a.z = __builtin_amdgcn_update_dpp(0u, a.x, 0x128, 0xf, 0xf, false);
        a.w = __builtin_amdgcn_update_dpp(0u, a.y, 0x128, 0xf, 0xf, false);
        f4 acc[8];
#pragma unroll
        for (int e = 0; e < 8; ++e) acc[e] = mfma(a, fr[e], f4{0.f, 0.f, 0.f, 0.f});
        if (lane < 32) {
            LAS unsigned char* dst = L + 32768 + u * 4096 + (g * 128 + c * 8) * 16;
#pragma unroll
            for (int e = 0; e < 8; ++e) *(LAS f4*)(dst + ((e ^ (c & 7)) * 16)) = acc[e];
        }
        asm volatile("s_waitcnt lgkmcnt(0)" ::: "memory");
        STAMP(5);
        __builtin_amdgcn_s_barrier();
        asm volatile("" ::: "memory");
        {
            const int t = u * 64 + lane;
            const LAS f4* src = (const LAS f4*)(L + 32768 + ((t & ~7) + ((t & 7) ^ ((t >> 3) & 7))) * 16);
            const f4 s = (src[0] + src[256]) + (src[512] + src[768]);
            __builtin_amdgcn_raw_buffer_store_b128(nosent(s), rs, E2_OFF + v * 4096 + t * 16, 0, AUX_SC1);
        }
        STAMP(6);
    }
    STAMP(7);
    if (PROBE_COND) {
        const unsigned long long until = tentry + 3000 + PROBE_EXTRA;
        while (rt() < until) {}
    }
}
}

extern "C" void kernel_launch(void* const* d_in, const int* in_sizes, int n_in, void* d_out, int out_size, void* d_ws, size_t ws_size,
                              hipStream_t stream) {
    swiglu_fused<<<NWG, NTHR, LDS_BYTES, stream>>>((const unsigned char*)d_in[0], (const unsigned char*)d_in[1], (const unsigned char*)d_in[2],
                                                   (const unsigned char*)d_in[3], (unsigned short*)d_out, (unsigned char*)d_ws);
}
```

```cpp
#include <hip/hip_runtime.h>

struct Tab_k { unsigned d[512]; };
static const Tab_k tab_k = {{
    0x00004000u, 0x08104010u, 0x08204808u, 0x08120042u, 0x00005000u, 0x0813c103u, 0x08205829u, 0x08158065u,
    0x00008000u, 0x08174087u, 0x0820884au, 0x081900a9u, 0x00009000u, 0x081ac10bu, 0x0820986bu, 0x081c80dcu,
    0x0000c000u, 0x081e40feu, 0x0820c88cu, 0x38704810u, 0x0000d000u, 0x38720842u, 0x0820d8adu, 0x3873c903u,
    0x00010000u, 0x38758865u, 0x082108ceu, 0x38774887u, 0x00020000u, 0x387908a9u, 0x082208efu, 0x387ac90bu,
    0x00011000u, 0x387c88dcu, 0x09011908u, 0x387e48feu, 0x00021000u, 0x08905010u, 0x09021929u, 0x08921042u,
    0x00014000u, 0x0893d103u, 0x0901494au, 0x08959065u, 0x00015000u, 0x08975087u, 0x0901596bu, 0x089910a9u,
    0x09e18188u, 0x089ad10bu, 0x09e189a9u, 0x089c90dcu, 0x09e191cau, 0x089e50feu, 0x09e199ebu, 0x10d05810u,
    0x0001c000u, 0x10d21842u, 0x0001c800u, 0x10d3d903u, 0x0001d000u, 0x10d59865u, 0x0001d800u, 0x10d75887u,
    0x00024000u, 0x10d918a9u, 0x09024a0cu, 0x10dad90bu, 0x00025000u, 0x10dc98dcu, 0x09025a2du, 0x10de58feu,
    0x00028000u, 0x09308010u, 0x09028a4eu, 0x09324042u, 0x00029000u, 0x09340103u, 0x09029a6fu, 0x0935c065u,
    0x0002c000u, 0x09378087u, 0x0ac2ca88u, 0x093940a9u, 0x0002d000u, 0x093b010bu, 0x0ac2daa9u, 0x093cc0dcu,
    0x00030000u, 0x093e80feu, 0x0ac30acau, 0x39908810u, 0x00031000u, 0x39924842u, 0x0ac31aebu, 0x39940903u,
    0x00034000u, 0x3995c865u, 0x0ac34b0cu, 0x39978887u, 0x00040000u, 0x399948a9u, 0x0ac40b2du, 0x399b090bu,
    0x00035000u, 0x399cc8dcu, 0x0ac35b4eu, 0x399e88feu, 0x00041000u, 0x09b09010u, 0x0ac41b6fu, 0x09b25042u,
    0x0ba38388u, 0x09b41103u, 0x0ba38ba9u, 0x09b5d065u, 0x0ba393cau, 0x09b79087u, 0x0ba39bebu, 0x09b950a9u,
    0x0003c000u, 0x09bb110bu, 0x0003c800u, 0x09bcd0dcu, 0x0003d000u, 0x09be90feu, 0x0003d800u, 0x40000000u,
    0x00044000u, 0x0a109810u, 0x0ba44c0cu, 0x0a125842u, 0x00045000u, 0x0a141903u, 0x0ba45c2du, 0x0a15d865u,
    0x00048000u, 0x0a179887u, 0x0ba48c4eu, 0x0a1958a9u, 0x00049000u, 0x0a1b190bu, 0x0ba49c6fu, 0x0a1cd8dcu,
    0x0004c000u, 0x0a1e98feu, 0x0c84cc88u, 0x0a50c010u, 0x0004d000u, 0x0a528042u, 0x0c84dca9u, 0x0a544103u,
    0x00050000u, 0x0a560065u, 0x0c850ccau, 0x0a57c087u, 0x00051000u, 0x0a5980a9u, 0x0c851cebu, 0x0a5b410bu,
    0x00054000u, 0x0a5d00dcu, 0x0c854d0cu, 0x0a5ec0feu, 0x00055000u, 0x3ab0c810u, 0x0c855d2du, 0x3ab28842u,
    0x00058000u, 0x3ab44903u, 0x0c858d4eu, 0x3ab60865u, 0x00060000u, 0x3ab7c887u, 0x0c860d6fu, 0x3ab988a9u,
    0x0d659588u, 0x3abb490bu, 0x0d659da9u, 0x3abd08dcu, 0x0d6615cau, 0x3abec8feu, 0x0d661debu, 0x12d0d010u,
    0x0005c000u, 0x12d29042u, 0x0005c800u, 0x12d45103u, 0x0005d000u, 0x12d61065u, 0x0005d800u, 0x12d7d087u,
    0x00064000u, 0x12d990a9u, 0x0e464e08u, 0x12db510bu, 0x00065000u, 0x12dd10dcu, 0x0e465e29u, 0x12ded0feu,
    0x00068000u, 0x0b30d810u, 0x0e468e4au, 0x0b329842u, 0x00069000u, 0x0b345903u, 0x0e469e6bu, 0x0b361865u,
    0x0006c000u, 0x0b37d887u, 0x0e46ce8cu, 0x0b3998a9u, 0x0006d000u, 0x0b3b590bu, 0x0e46deadu, 0x0b3d18dcu,
    0x00070000u, 0x0b3ed8feu, 0x0e470eceu, 0x0b710010u, 0x00071000u, 0x0b72c042u, 0x0e471eefu, 0x0b748103u,
    0x00074000u, 0x0b764065u, 0x0f274f08u, 0x0b780087u, 0x00075000u, 0x0b79c0a9u, 0x0f275f29u, 0x0b7b810bu,
    0x00078000u, 0x0b7d40dcu, 0x0f278f4au, 0x0b7f00feu, 0x00079000u, 0x3bd10810u, 0x0f279f6bu, 0x3bd2c842u,
    0x0f27c78cu, 0x3bd48903u, 0x0f27cfadu, 0x3bd64865u, 0x0f2807ceu, 0x3bd80887u, 0x0f280fffu, 0x3bd9c8a9u,
    0x0007d000u, 0x3bdb890bu, 0x0007d800u, 0x3bdd48dcu, 0x00081000u, 0x3bdf08feu, 0x00081800u, 0x40000000u,
    0x00084000u, 0x0c111010u, 0x08284808u, 0x0c12d042u, 0x00085000u, 0x0c149103u, 0x08285829u, 0x0c165065u,
    0x00088000u, 0x0c181087u, 0x0828884au, 0x0c19d0a9u, 0x00089000u, 0x0c1b910bu, 0x0828986bu, 0x0c1d50dcu,
    0x0008c000u, 0x0c511810u, 0x0828c88cu, 0x0c52d842u, 0x0008d000u, 0x0c549903u, 0x0828d8adu, 0x0c565865u,
    0x00090000u, 0x0c581887u, 0x082908ceu, 0x0c59d8a9u, 0x00091000u, 0x0c5b990bu, 0x082918efu, 0x0c5d58dcu,
    0x00094000u, 0x0c914010u, 0x09094908u, 0x0c930042u, 0x00095000u, 0x0c94c103u, 0x09095929u, 0x0c968065u,
    0x00098000u, 0x0c984087u, 0x0909894au, 0x0c9a00a9u, 0x00099000u, 0x0c9bc10bu, 0x0909996bu, 0x0c9d80dcu,
    0x09e9c188u, 0x1cd14810u, 0x09e9c9a9u, 0x1cd30842u, 0x09e9d1cau, 0x1cd4c903u, 0x09e9d9ebu, 0x1cd68865u,
    0x40000000u, 0x40000000u, 0x40000000u, 0x0c1f10feu, 0x40000000u, 0x0c5f18feu, 0x40000000u, 0x0c9f40feu,
    0x000a4000u, 0x1cd84887u, 0x090a4a0cu, 0x1cda08a9u, 0x000c0000u, 0x1cdbc90bu, 0x090c0a2du, 0x1cdd88dcu,
    0x000a5000u, 0x1cdf48feu, 0x090a5a4eu, 0x15115010u, 0x000c1000u, 0x15131042u, 0x090c1a6fu, 0x1514d103u,
    0x000a8000u, 0x15169065u, 0x0aca8a88u, 0x15185087u, 0x000a9000u, 0x151a10a9u, 0x0aca9aa9u, 0x151bd10bu,
    0x000ac000u, 0x151d90dcu, 0x0acacacau, 0x151f50feu, 0x000ad000u, 0x0d715810u, 0x0acadaebu, 0x0d731842u,
    0x000b0000u, 0x0d74d903u, 0x0acb0b0cu, 0x0d769865u, 0x000b1000u, 0x0d785887u, 0x0acb1b2du, 0x0d7a18a9u,
    0x000b4000u, 0x0d7bd90bu, 0x0acb4b4eu, 0x0d7d98dcu, 0x000b5000u, 0x0d7f58feu, 0x0acb5b6fu, 0x0db18010u,
    0x0bab8388u, 0x0db34042u, 0x0bab8ba9u, 0x0db50103u, 0x0bab93cau, 0x0db6c065u, 0x0bab9bebu, 0x0db88087u,
    0x000bc000u, 0x0dba40a9u, 0x000bc800u, 0x0dbc010bu, 0x000bd000u, 0x0dbdc0dcu, 0x000bd800u, 0x0dbf80feu,
    0x000c4000u, 0x3e318810u, 0x0bac4c0cu, 0x3e334842u, 0x000c5000u, 0x3e350903u, 0x0bac5c2du, 0x3e36c865u,
    0x000c8000u, 0x3e388887u, 0x0bac8c4eu, 0x3e3a48a9u, 0x000e0000u, 0x3e3c090bu, 0x0bae0c6fu, 0x3e3dc8dcu,
    0x000c9000u, 0x3e3f88feu, 0x0c8c9c88u, 0x0e519010u, 0x000e1000u, 0x0e535042u, 0x0c8e1ca9u, 0x0e551103u,
    0x000cc000u, 0x0e56d065u, 0x0c8ccccau, 0x0e589087u, 0x000cd000u, 0x0e5a50a9u, 0x0c8cdcebu, 0x0e5c110bu,
    0x000d0000u, 0x0e5dd0dcu, 0x0c8d0d0cu, 0x0e5f90feu, 0x000d1000u, 0x0e919810u, 0x0c8d1d2du, 0x0e935842u,
    0x000d4000u, 0x0e951903u, 0x0c8d4d4eu, 0x0e96d865u, 0x000d5000u, 0x0e989887u, 0x0c8d5d6fu, 0x0e9a58a9u,
    0x0d6d8588u, 0x0e9c190bu, 0x0d6d8da9u, 0x0e9dd8dcu, 0x0d6d95cau, 0x0e9f98feu, 0x0d6d9debu, 0x16d1c010u,
    0x000dc000u, 0x16d38042u, 0x000dc800u, 0x16d54103u, 0x000dd000u, 0x16d70065u, 0x000dd800u, 0x16d8c087u,
    0x000e4000u, 0x16da80a9u, 0x0e4e4e08u, 0x16dc410bu, 0x000e5000u, 0x16de00dcu, 0x0e4e5e29u, 0x16dfc0feu,
    0x000e8000u, 0x3f51c810u, 0x0e4e8e4au, 0x3f538842u, 0x000e9000u, 0x3f554903u, 0x0e4e9e6bu, 0x3f570865u,
    0x000ec000u, 0x3f58c887u, 0x0e4ece8cu, 0x3f5a88a9u, 0x00100000u, 0x3f5c490bu, 0x0e500eadu, 0x3f5e08dcu,
    0x000ed000u, 0x3f5fc8feu, 0x0e4edeceu, 0x0f71d010u, 0x00101000u, 0x0f739042u, 0x0e501eefu, 0x0f755103u,
    0x000f0000u, 0x0f771065u, 0x0f2f0f08u, 0x0f78d087u, 0x000f1000u, 0x0f7a90a9u, 0x0f2f1f29u, 0x0f7c510bu,
    0x000f4000u, 0x0f7e10dcu, 0x0f2f4f4au, 0x0f7fd0feu, 0x000f5000u, 0x0fb1d810u, 0x0f2f5f6bu, 0x0fb39842u,
    0x0f2f878cu, 0x0fb55903u, 0x0f2f8fadu, 0x0fb71865u, 0x0f2f97ceu, 0x0fb8d887u, 0x0f2f9fffu, 0x0fba98a9u,
    0x000fc000u, 0x0fbc590bu, 0x000fc800u, 0x0fbe18dcu, 0x000fd000u, 0x0fbfd8feu, 0x000fd800u, 0x40000000u
}};

__global__ void __launch_bounds__(1024, 4) __attribute__((aligned(4096)))
swiglu_k(const void* x, const void* w1, const void* w3, const void* w2, void* out, void* ws, Tab_k tab) {
    asm volatile(
        "s_load_dwordx4 s[4:7], %[ka], 0x8\n"
        "s_load_dwordx2 s[28:29], %[ka], 0x28\n"
        "s_load_dwordx2 s[24:25], %[ka], 0x0\n"
        "v_readfirstlane_b32 s15, v0\n"
        "s_and_b32 s13, %[bid], 7\n"
        "s_and_b32 s19, %[bid], 0xf8\n"
        "s_lshl_b32 s18, s13, 8\n"
        "s_add_u32 s18, s18, s19\n"
        "s_mov_b32 s52, 4194304\n"
        "s_mov_b32 s30, 2097152\n"
        "s_mov_b32 s31, 0x20000\n"
        "s_mov_b32 s23, s31\n"
        "s_mov_b32 s22, 7340032\n"
        "s_movk_i32 s26, 0x4000\n"
        "s_mov_b32 s27, s31\n"
        "s_mov_b32 s44, 0x05040100\n"
        "s_mov_b32 s45, 0x07060302\n"
        "v_and_b32 v1, 63, v0\n"
        "v_bfe_u32 v2, v0, 4, 2\n"
        "v_and_b32 v7, 7, v0\n"
        "v_mov_b32 v127, 0\n"
        "s_lshr_b32 s15, s15, 6\n"
        "s_cmp_gt_u32 s15, 7\n"
        "s_cbranch_scc1 .Lk_BH\n"
        "s_mov_b32 s19, 57344\n"
        "v_lshrrev_b32 v3, 3, v0\n"
        "v_lshlrev_b32 v4, 4, v7\n"
        "v_mad_u32_u24 v4, v3, s19, v4\n"
        "s_add_u32 s46, s13, 3\n"
        "s_and_b32 s46, s46, 7\n"
        "s_lshr_b32 s47, %[bid], 5\n"
        "s_sub_u32 s48, s47, s46\n"
        "s_cmp_gt_u32 s47, s46\n"
        "s_subb_u32 s47, s47, 0\n"
        "s_mul_i32 s49, s13, 7\n"
        "s_add_u32 s47, s47, s49\n"
        "s_lshl_b32 s34, s47, 7\n"
        "s_bfe_u32 s49, %[bid], 0x10003\n"
        "s_lshl_b32 s49, s49, 1\n"
        "s_bfe_u32 s50, %[bid], 0x20003\n"
        "s_bfe_u32 s51, %[bid], 0x10004\n"
        "s_lshr_b32 s35, s15, 2\n"
        "s_lshl_b32 s35, s35, 30\n"
        "s_cmp_lt_u32 s48, 2\n"
        "s_cselect_b32 s49, s50, s49\n"
        "s_cselect_b32 s51, s48, s51\n"
        "s_cselect_b32 s35, s35, 0\n"
        "s_mul_i32 s48, s49, 0x1c0000\n"
        "s_add_u32 s34, s34, s48\n"
        "s_add_u32 s34, s34, s35\n"
        "s_lshl_b32 s42, s49, 9\n"
        "s_add_u32 s42, s42, s35\n"
        "s_and_b32 s43, %[bid], 0xe7\n"
        "s_cmpk_eq_u32 s43, 0xe4\n"
        "s_cbranch_scc1 .Lk_CAL\n"
        "s_cmp_lg_u32 s51, 0\n"
        "s_setprio 3\n"
        "s_waitcnt lgkmcnt(0)\n"
        "s_cselect_b64 s[20:21], s[6:7], s[4:5]\n"
        "buffer_load_dwordx4 v[8:11], v4, s[20:23], s34 offen nt\n"
        "s_setprio 0\n"
        "s_addk_i32 s34, 0x1c00\n"
        "s_load_dword s8, s[28:29], 0x1020\n"
        "s_load_dword s16, %[ka], s18 offset:48\n"
        "buffer_load_dwordx4 v[12:15], v4, s[20:23], s34 offen nt\n"
        "s_addk_i32 s34, 0x1c00\n"
        "buffer_load_dwordx4 v[16:19], v4, s[20:23], s34 offen nt\n"
        "s_addk_i32 s34, 0x1c00\n"
        "buffer_load_dwordx4 v[20:23], v4, s[20:23], s34 offen nt\n"
        "s_addk_i32 s34, 0x1c00\n"
        "buffer_load_dwordx4 v[24:27], v4, s[20:23], s34 offen nt\n"
        "s_addk_i32 s34, 0x1c00\n"
        "buffer_load_dwordx4 v[28:31], v4, s[20:23], s34 offen nt\n"
        "s_addk_i32 s34, 0x1c00\n"
        "buffer_load_dwordx4 v[32:35], v4, s[20:23], s34 offen nt\n"
        "s_addk_i32 s34, 0x1c00\n"
        "buffer_load_dwordx4 v[36:39], v4, s[20:23], s34 offen nt\n"
        "s_branch .Lk_A2\n"
        ".Lk_CAL:\n"
        "s_cmp_lg_u32 s15, 0\n"
        "s_cbranch_scc1 .Lk_END\n"
        "s_load_dwordx2 s[38:39], %[ka], 0x20\n"
        "s_mul_i32 s56, s50, 0x2100\n"
        "s_and_b32 s56, s56, 0x2200\n"
        "s_and_b32 s55, s56, 0x2000\n"
        "v_lshlrev_b32 v12, 7, v1\n"
        "s_mov_b64 exec, 0x1ffff\n"
        "s_lshl_b32 s57, s50, 3\n"
        "s_waitcnt lgkmcnt(0)\n"
        "s_memtime s[40:41]\n"
        "s_load_dword s8, s[28:29], 0x1020\n"
        "s_load_dword s46, s[28:29], s56 offset:0x1900 glc\n"
        "s_load_dword s46, s[28:29], s56 offset:0x1a00 glc\n"
        "s_load_dword s46, s[38:39], s56 offset:0x1900 glc\n"
        "s_load_dword s46, s[38:39], s56 offset:0x1a00 glc\n"
        "buffer_load_dword v8, v12, s[28:31], s55 offen offset:2048 sc1\n"
        "s_mov_b64 s[4:5], s[28:29]\n"
        "s_mov_b32 s54, -1\n"
        "s_movk_i32 s53, 5\n"
        "s_waitcnt lgkmcnt(0)\n"
        "s_add_u32 s8, s8, 0x01010101\n"
        "v_mov_b32 v10, s8\n"
        ".Lk_CAL1:\n"
        "s_memtime s[46:47]\n"
        "s_waitcnt lgkmcnt(0)\n"
        "s_load_dword s51, s[4:5], s56 offset:0x1900 glc\n"
        "s_waitcnt lgkmcnt(0)\n"
        "s_memtime s[48:49]\n"
        "s_xor_b32 s56, s56, 0x100\n"
        "s_waitcnt lgkmcnt(0)\n"
        "s_sub_u32 s46, s48, s46\n"
        "s_min_u32 s54, s46, s54\n"
        "s_sub_u32 s48, s48, s40\n"
        "s_cmpk_gt_i32 s48, 4500\n"
        "s_cselect_b32 s53, 0, s53\n"
        "s_sub_u32 s53, s53, 1\n"
        "s_cbranch_scc0 .Lk_CAL1\n"
        "v_mov_b32 v11, s54\n"
        "s_barrier\n"
        "buffer_store_dwordx2 v[10:11], v12, s[28:31], s57 offen offset:2048 sc1\n"
        "s_bitcmp1_b32 s57, 13\n"
        "s_cbranch_scc1 .Lk_END\n"
        "s_addk_i32 s40, 900\n"
        "s_add_u32 s57, s57, 0x80001000\n"
        "s_cselect_b32 s54, s54, -1\n"
        "s_cselect_b32 s53, 1, 0\n"
        "s_mov_b64 s[4:5], s[38:39]\n"
        "s_branch .Lk_CAL1\n"
        ".Lk_A2:\n"
        "v_bfe_u32 v5, v0, 2, 2\n"
        "v_and_b32 v6, 1, v0\n"
        "v_lshl_add_u32 v5, v5, 1, v6\n"
        "v_lshrrev_b32 v6, 4, v0\n"
        "v_lshlrev_b32 v6, 5, v6\n"
        "v_lshl_add_u32 v5, v5, 11, v6\n"
        "v_and_b32 v6, 2, v0\n"
        "v_lshl_add_u32 v5, v6, 3, v5\n"
        "v_lshlrev_b32 v119, 2, v1\n"
        "v_lshlrev_b32 v3, 4, v1\n"
        "v_lshlrev_b32 v4, 5, v2\n"
        "s_lshl_b32 s47, s15, 11\n"
        "v_xad_u32 v116, v3, v4, s47\n"
        "v_xor_b32 v117, 16, v116\n"
        "v_bfe_u32 v2, v0, 2, 3\n"
        "v_bfe_u32 v3, v0, 5, 1\n"
        "v_lshl_add_u32 v3, v3, 3, s15\n"
        "v_xor_b32 v3, v3, v2\n"
        "v_and_b32 v4, 1, v2\n"
        "v_lshl_add_u32 v3, v4, 6, v3\n"
        "v_lshrrev_b32 v4, 1, v2\n"
        "v_lshl_add_u32 v3, v4, 4, v3\n"
        "v_and_b32 v4, 12, v119\n"
        "v_lshl_add_u32 v118, v3, 4, v4\n"
        "s_lshl_b32 s46, s15, 8\n"
        "v_lshlrev_b32 v4, 1, v4\n"
        "s_movk_i32 s14, 0xff00\n"
        "s_waitcnt lgkmcnt(0)\n"
        "s_and_b32 s47, s16, 0x401FF800\n"
        "s_add_u32 s47, s47, s46\n"
        "s_add_u32 s8, s8, 0x01010101\n"
        "v_bfe_u32 v120, s8, v4, 8\n"
        "s_barrier\n"
        "buffer_load_dwordx4 v[40:43], v5, s[24:27], s42 offen\n"
        "s_waitcnt vmcnt(7)\n"
        "v_perm_b32 v44, v12, v8, s44\n"
        "v_perm_b32 v48, v12, v8, s45\n"
        "v_perm_b32 v52, v13, v9, s44\n"
        "v_perm_b32 v56, v13, v9, s45\n"
        "v_perm_b32 v60, v14, v10, s44\n"
        "v_perm_b32 v64, v14, v10, s45\n"
        "v_perm_b32 v68, v15, v11, s44\n"
        "v_perm_b32 v72, v15, v11, s45\n"
        "s_waitcnt vmcnt(5)\n"
        "v_perm_b32 v45, v20, v16, s44\n"
        "v_perm_b32 v49, v20, v16, s45\n"
        "v_perm_b32 v53, v21, v17, s44\n"
        "v_perm_b32 v57, v21, v17, s45\n"
        "v_perm_b32 v61, v22, v18, s44\n"
        "v_perm_b32 v65, v22, v18, s45\n"
        "v_perm_b32 v69, v23, v19, s44\n"
        "v_perm_b32 v73, v23, v19, s45\n"
        "s_waitcnt vmcnt(3)\n"
        "v_perm_b32 v46, v28, v24, s44\n"
        "v_perm_b32 v50, v28, v24, s45\n"
        "v_perm_b32 v54, v29, v25, s44\n"
        "v_perm_b32 v58, v29, v25, s45\n"
        "v_perm_b32 v62, v30, v26, s44\n"
        "v_perm_b32 v66, v30, v26, s45\n"
        "v_perm_b32 v70, v31, v27, s44\n"
        "v_perm_b32 v74, v31, v27, s45\n"
        "s_waitcnt vmcnt(1)\n"
        "s_barrier\n"
        "v_perm_b32 v47, v36, v32, s44\n"
        "v_perm_b32 v51, v36, v32, s45\n"
        "v_perm_b32 v55, v37, v33, s44\n"
        "v_perm_b32 v59, v37, v33, s45\n"
        "v_perm_b32 v63, v38, v34, s44\n"
        "v_perm_b32 v67, v38, v34, s45\n"
        "v_perm_b32 v71, v39, v35, s44\n"
        "v_perm_b32 v75, v39, v35, s45\n"
        "s_waitcnt vmcnt(0)\n"
        "v_mfma_f32_16x16x32_bf16 v[76:79], v[40:43], v[44:47], 0\n"
        "v_mfma_f32_16x16x32_bf16 v[92:95], v[40:43], v[60:63], 0\n"
        "v_mfma_f32_16x16x32_bf16 v[80:83], v[40:43], v[48:51], 0\n"
        "v_mfma_f32_16x16x32_bf16 v[96:99], v[40:43], v[64:67], 0\n"
        "v_mfma_f32_16x16x32_bf16 v[84:87], v[40:43], v[52:55], 0\n"
        "v_mfma_f32_16x16x32_bf16 v[100:103], v[40:43], v[68:71], 0\n"
        "v_mfma_f32_16x16x32_bf16 v[88:91], v[40:43], v[56:59], 0\n"
        "v_mfma_f32_16x16x32_bf16 v[104:107], v[40:43], v[72:75], 0\n"
        "s_nop 1\n"
        "v_add_f32_dpp v108, v78, v76 row_ror:8 row_mask:0xf bank_mask:0x3\n"
        "v_add_f32_dpp v108, v92, v94 row_ror:8 row_mask:0xf bank_mask:0xc\n"
        "v_add_f32_dpp v112, v79, v77 row_ror:8 row_mask:0xf bank_mask:0x3\n"
        "v_add_f32_dpp v112, v93, v95 row_ror:8 row_mask:0xf bank_mask:0xc\n"
        "v_add_f32_dpp v109, v82, v80 row_ror:8 row_mask:0xf bank_mask:0x3\n"
        "v_add_f32_dpp v109, v96, v98 row_ror:8 row_mask:0xf bank_mask:0xc\n"
        "v_add_f32_dpp v113, v83, v81 row_ror:8 row_mask:0xf bank_mask:0x3\n"
        "v_add_f32_dpp v113, v97, v99 row_ror:8 row_mask:0xf bank_mask:0xc\n"
        "v_add_f32_dpp v110, v86, v84 row_ror:8 row_mask:0xf bank_mask:0x3\n"
        "v_add_f32_dpp v110, v100, v102 row_ror:8 row_mask:0xf bank_mask:0xc\n"
        "v_add_f32_dpp v114, v87, v85 row_ror:8 row_mask:0xf bank_mask:0x3\n"
        "v_add_f32_dpp v114, v101, v103 row_ror:8 row_mask:0xf bank_mask:0xc\n"
        "v_add_f32_dpp v111, v90, v88 row_ror:8 row_mask:0xf bank_mask:0x3\n"
        "v_add_f32_dpp v111, v104, v106 row_ror:8 row_mask:0xf bank_mask:0xc\n"
        "v_add_f32_dpp v115, v91, v89 row_ror:8 row_mask:0xf bank_mask:0x3\n"
        "v_add_f32_dpp v115, v105, v107 row_ror:8 row_mask:0xf bank_mask:0xc\n"
        "ds_write_b128 v116, v[108:111]\n"
        "ds_write_b128 v117, v[112:115] offset:1024\n"
        "s_waitcnt lgkmcnt(0)\n"
        "s_barrier\n"
        "ds_read2st64_b32 v[8:9], v118 offset0:0 offset1:8\n"
        "ds_read2st64_b32 v[10:11], v118 offset0:16 offset1:24\n"
        "ds_read2st64_b32 v[12:13], v118 offset0:32 offset1:40\n"
        "ds_read2st64_b32 v[14:15], v118 offset0:48 offset1:56\n"
        "s_waitcnt lgkmcnt(2)\n"
        "v_pk_add_f32 v[8:9], v[8:9], v[10:11]\n"
        "s_waitcnt lgkmcnt(0)\n"
        "v_pk_add_f32 v[12:13], v[12:13], v[14:15]\n"
        "v_pk_add_f32 v[8:9], v[8:9], v[12:13]\n"
        "v_add_f32 v8, v8, v9\n"
        "v_and_or_b32 v8, v8, s14, v120\n"
        "s_movk_i32 s46, 3\n"
        ".Lk_A3:\n"
        "ds_read_b64 v[16:17], v127 offset:32768\n"
        "s_sub_u32 s46, s46, 1\n"
        "s_waitcnt lgkmcnt(0)\n"
        "v_cmp_gt_u32 vcc, s26, v17\n"
        "v_add_u32 v16, v119, v16\n"
        "v_add_u32 v17, v119, v17\n"
        "s_cbranch_scc1 .Lk_A4\n"
        "s_cbranch_vccnz .Lk_A3\n"
        ".Lk_A4:\n"
        "buffer_store_dword v8, v16, s[28:31], s47 offen sc1\n"
        "buffer_store_dword v8, v17, s[28:31], s47 offen sc1\n"
        ".Lk_END:\n"
        "s_endpgm\n"
        ".Lk_BH:\n"
        "s_memrealtime s[42:43]\n"
        "s_load_dwordx4 s[36:39], %[ka], 0x18\n"
        "s_load_dwordx2 s[16:17], %[ka], s18 offset:48\n"
        "s_lshl_b32 s59, s13, 7\n"
        "s_addk_i32 s59, 2048\n"
        "s_and_b32 s58, %[bid], 4\n"
        "s_lshl_b32 s58, s58, 11\n"
        "s_and_b32 s46, s15, 1\n"
        "v_and_b32 v6, 3, v0\n"
        "v_lshlrev_b32 v5, 3, v6\n"
        "s_waitcnt lgkmcnt(0)\n"
        "s_load_dword s8, s[28:29], 0x1020\n"
        "s_and_b32 s53, s17, 0x401FF800\n"
        "s_lshl_b32 s57, s46, 10\n"
        "s_add_u32 s53, s53, s57\n"
        "s_lshr_b32 s48, s17, 8\n"
        "s_and_b32 s48, s48, s46\n"
        "s_cselect_b32 s53, s30, s53\n"
        "s_and_b32 s48, s15, 2\n"
        "s_lshl_b32 s48, s48, 12\n"
        "s_cmp_lt_u32 s15, 12\n"
        "s_cbranch_scc1 .Lk_B\n"
        "s_add_u32 s53, s53, s48\n"
        "s_and_b32 s34, s15, 3\n"
        "s_lshl1_add_u32 s19, s34, s19\n"
        "s_lshl_b32 s49, s19, 6\n"
        "buffer_load_dword v8, off, s[24:27], s49\n"
        "s_mov_b64 s[24:25], s[38:39]\n"
        "s_and_b32 s49, s16, 0x401FF800\n"
        "s_add_u32 s48, s48, s57\n"
        "s_add_u32 s48, s48, s49\n"
        "v_lshlrev_b32 v4, 7, v7\n"
        "buffer_load_dword v8, v4, s[28:31], s48 offen sc1\n"
        "v_and_b32 v3, 4, v0\n"
        "v_lshl_add_u32 v5, v3, 11, v5\n"
        "v_bfe_u32 v3, v0, 2, 2\n"
        "v_lshlrev_b32 v16, 4, v6\n"
        "v_lshl_add_u32 v16, v3, 11, v16\n"
        "v_lshl_add_u32 v16, v2, 14, v16\n"
        "v_lshlrev_b32 v17, 1, v6\n"
        "v_lshl_add_u32 v17, v2, 11, v17\n"
        "s_bfe_u32 s54, s16, 0x70015\n"
        "s_lshl_b32 s54, s54, 6\n"
        "s_and_b32 s55, s16, 7\n"
        "s_add_u32 s54, s54, s55\n"
        "s_lshl2_add_u32 s54, s54, s34\n"
        "s_lshl_b32 s54, s54, 6\n"
        "s_add_u32 s49, s54, 0x10000\n"
        "s_and_b32 s55, s16, 0x7e0\n"
        "s_lshl3_add_u32 s55, s34, s55\n"
        ".Lk_TAG:\n"
        "s_waitcnt lgkmcnt(0)\n"
        "s_add_u32 s8, s8, 0x01010101\n"
        "s_and_b32 s9, s8, 0xff\n"
        "s_bfe_u32 s10, s8, 0x80008\n"
        "s_bfe_u32 s11, s8, 0x80010\n"
        "s_lshr_b32 s12, s8, 24\n"
        "s_movk_i32 s19, 0x2000\n"
        "s_cmp_lt_u32 s15, 12\n"
        "s_barrier\n"
        "s_sleep 3\n"
        "s_cbranch_scc1 .Lk_B1\n"
        "buffer_load_dword v8, v4, s[28:31], s53 offen sc1\n"
        "v_mov_b32 v14, 0\n"
        "v_mov_b32 v15, s19\n"
        "ds_write_b64 v127, v[14:15] offset:32768\n"
        "v_mov_b32 v20, s9\n"
        "v_mov_b32 v21, s10\n"
        "v_mov_b32 v22, s11\n"
        "v_mov_b32 v23, s12\n"
        "v_not_b32 v24, s9\n"
        "v_not_b32 v25, s10\n"
        "v_not_b32 v26, s11\n"
        "v_not_b32 v27, s12\n"
        "s_mov_b32 s47, 0xffff\n"
        "s_mov_b32 exec_hi, s47\n"
        "v_mov_b64 v[20:21], v[24:25]\n"
        "v_mov_b64 v[22:23], v[26:27]\n"
        "s_mov_b32 exec_hi, -1\n"
        "s_cmp_eq_u32 s15, 15\n"
        "s_cselect_b32 s14, s16, 0\n"
        "s_bitcmp1_b32 s14, 4\n"
        "s_cselect_b32 s14, 0x1020, s30\n"
        "s_waitcnt lgkmcnt(0)\n"
        "s_barrier\n"
        "s_barrier\n"
        "s_sleep 20\n"
        ".Lk_H1:\n"
        "buffer_load_dwordx2 v[10:11], v5, s[28:31], s59 offen offset:1024 sc1\n"
        "s_waitcnt vmcnt(0)\n"
        "v_cmp_ne_u32 vcc, s8, v10\n"
        "v_min_u32_dpp v11, v11, v11 quad_perm:[2,3,0,1] row_mask:0xf bank_mask:0xf\n"
        "s_sub_u32 s52, s52, 1\n"
        "s_cbranch_scc1 .Lk_H4\n"
        "s_cbranch_vccnz .Lk_H1\n"
        ".Lk_H4:\n"
        "v_sub_u32_dpp v11, v11, v11 quad_perm:[1,0,3,2] row_mask:0xf bank_mask:0xf\n"
        "s_max_i32 s52, s52, 0\n"
        "v_ashrrev_i32 v11, 31, v11\n"
        "v_xor_b32 v11, s58, v11\n"
        "v_and_b32 v11, s19, v11\n"
        "s_nop 0\n"
        "v_readlane_b32 s4, v11, 1\n"
        "v_readlane_b32 s5, v11, 5\n"
        "s_nop 0\n"
        "v_add_u32 v16, s4, v16\n"
        "v_mov_b32 v12, s5\n"
        "ds_write_b32 v127, v12 offset:32776\n"
        "s_lshl_b32 s4, s4, 8\n"
        "s_waitcnt lgkmcnt(0)\n"
        "s_bitcmp0_b32 s16, 3\n"
        "s_cbranch_scc1 .Lk_END\n"
        "v_add_u32 v17, v17, v12\n"
        "s_add_u32 s14, s14, s4\n"
        "s_barrier\n"
        "s_sleep 12\n"
        ".Lk_H7:\n"
        "s_mov_b32 exec_hi, s47\n"
        "buffer_load_dwordx4 v[20:23], v16, s[28:31], s49 offen sc1\n"
        "s_mov_b32 exec_hi, -1\n"
        "buffer_load_dwordx4 v[24:27], v16, s[28:31], s54 offen sc1\n"
        "s_waitcnt vmcnt(6)\n"
        "v_xor_b32 v44, s9, v20\n"
        "v_xor_b32 v45, s10, v21\n"
        "v_xor_b32 v46, s11, v22\n"
        "v_xor_b32 v47, s12, v23\n"
        "v_xor_b32 v48, s9, v24\n"
        "v_xor_b32 v49, s10, v25\n"
        "v_xor_b32 v50, s11, v26\n"
        "v_xor_b32 v51, s12, v27\n"
        "v_or3_b32 v52, v44, v45, v46\n"
        "v_or3_b32 v52, v47, v48, v52\n"
        "v_or3_b32 v52, v49, v50, v52\n"
        "v_or_b32 v52, v51, v52\n"
        "v_cmp_ne_u32_sdwa vcc, v52, 0 src0_sel:BYTE_0 src1_sel:DWORD\n"
        "v_pk_add_f32 v[48:49], v[48:49], v[44:45]\n"
        "v_pk_add_f32 v[50:51], v[50:51], v[46:47]\n"
        "s_sub_u32 s52, s52, 1\n"
        "s_cbranch_scc1 .Lk_H8\n"
        "s_cbranch_vccnz .Lk_H7\n"
        ".Lk_H8:\n"
        "v_permlane32_swap_b32 v48, v50\n"
        "v_permlane32_swap_b32 v49, v51\n"
        "v_pk_add_f32 v[48:49], v[48:49], v[50:51]\n"
        "s_nop 1\n"
        "v_permlane16_swap_b32 v48, v49\n"
        "v_add_f32 v48, v48, v49\n"
        "s_nop 1\n"
        "v_add_f32_dpp v48, v48, v48 row_ror:8 row_mask:0xf bank_mask:0xf\n"
        "s_nop 1\n"
        "v_add_f32_dpp v48, v48, v48 row_ror:4 row_mask:0xf bank_mask:0xf\n"
        "v_cvt_pk_bf16_f32 v48, v48, v48\n"
        "buffer_store_short v48, v17, s[24:27], s55 offen\n"
        "buffer_store_dword v10, off, s[28:31], s14 sc1\n"
        "s_endpgm\n"
        ".Lk_B:\n"
        "s_bitcmp1_b32 s17, 30\n"
        "s_cbranch_scc1 .Lk_END\n"
        "s_mov_b64 s[20:21], s[36:37]\n"
        "s_and_b32 s34, s15, 3\n"
        "s_mov_b32 s56, 0xbfb8aa3b\n"
        "v_and_b32 v3, 15, v0\n"
        "v_lshlrev_b32 v117, 4, v1\n"
        "v_lshlrev_b32 v4, 14, v2\n"
        "v_lshl_add_u32 v4, v7, 4, v4\n"
        "v_and_b32 v6, 2, v2\n"
        "v_lshlrev_b32 v6, 1, v6\n"
        "v_xor_b32 v6, v6, v7\n"
        "v_lshl_add_u32 v6, v3, 3, v6\n"
        "v_and_b32 v2, 1, v2\n"
        "v_lshl_add_u32 v6, v2, 7, v6\n"
        "v_lshl_add_u32 v4, v2, 13, v4\n"
        "v_lshlrev_b32 v7, 7, v2\n"
        "v_xor_b32 v7, v7, v117\n"
        "v_xor_b32 v117, s48, v117\n"
        "s_lshl_b32 s48, s34, 12\n"
        "v_lshl_add_u32 v120, v6, 4, s48\n"
        "v_xor_b32 v121, 16, v120\n"
        "v_xor_b32 v122, 32, v120\n"
        "v_xor_b32 v123, 48, v120\n"
        "v_and_b32 v6, 0xff, v0\n"
        "v_bfe_u32 v2, v6, 3, 3\n"
        "v_xor_b32 v6, v6, v2\n"
        "v_lshlrev_b32 v116, 4, v6\n"
        "v_mov_b64 v[92:93], 0\n"
        "v_mov_b64 v[94:95], 0\n"
        "v_mov_b64 v[100:101], 0\n"
        "v_mov_b64 v[102:103], 0\n"
        "v_mov_b64 v[108:109], 0\n"
        "v_mov_b64 v[110:111], 0\n"
        "v_mov_b64 v[112:113], 0\n"
        "v_mov_b64 v[114:115], 0\n"
        "v_mov_b32 v118, 0xffffff00\n"
        "v_mov_b32 v119, 0\n"
        "v_cmp_gt_u32 vcc, 8, v3\n"
        "s_bfe_u32 s35, s17, 0x60015\n"
        "s_bfe_i32 s47, s17, 0x3001b\n"
        "s_bitcmp1_b32 s34, 1\n"
        "s_cselect_b32 s47, s47, 0\n"
        "s_add_u32 s35, s35, s47\n"
        "s_lshr_b32 s47, s35, 3\n"
        "s_sub_u32 s34, s35, s47\n"
        "s_lshl1_add_u32 s47, s34, s46\n"
        "s_lshl_b32 s47, s47, 16\n"
        "s_and_b32 s49, s17, 15\n"
        "s_lshl_b32 s49, s49, 7\n"
        "s_and_b32 s50, s17, 0xf0\n"
        "s_lshl_b32 s50, s50, 3\n"
        "s_bitcmp1_b32 s17, 8\n"
        "s_cselect_b32 s50, s22, s50\n"
        "v_mov_b32 v2, s49\n"
        "v_mov_b32 v3, s50\n"
        "v_cndmask_b32 v2, v3, v2, vcc\n"
        "v_add_u32 v4, v4, v2\n"
        "v_xor_b32 v6, 0x2000, v4\n"
        "s_addk_i32 s57, 0x4000\n"
        "s_lshl_b32 s50, s35, 14\n"
        "s_or_b32 s48, s50, 0x1c000\n"
        "s_add_u32 s50, s50, s57\n"
        "s_add_u32 s51, s50, 0x1000\n"
        "s_add_u32 s48, s48, s57\n"
        "s_add_u32 s49, s48, 0x1000\n"
        "s_and_b32 s46, s34, 7\n"
        "s_branch .Lk_TAG\n"
        ".Lk_B5:\n"
        "s_sub_u32 s52, s52, 1\n"
        "s_cbranch_scc0 .Lk_B3\n"
        "s_branch .Lk_B4\n"
        ".Lk_B7:\n"
        "s_sub_u32 s52, s52, 1\n"
        "s_cbranch_scc1 .Lk_B9\n"
        "buffer_load_dwordx2 v[2:3], v5, s[28:31], s59 offen sc1\n"
        "s_waitcnt vmcnt(0)\n"
        "s_branch .Lk_B2\n"
        ".Lk_B9:\n"
        "s_mov_b32 s52, 0\n"
        "s_branch .Lk_B8\n"
        ".Lk_B1:\n"
        "buffer_load_dwordx4 v[8:11], v4, s[20:23], s47 offen nt\n"
        "buffer_load_dwordx4 v[12:15], v4, s[20:23], s47 offen offset:2048 nt\n"
        "s_addk_i32 s47, 0x1000\n"
        "buffer_load_dwordx4 v[16:19], v4, s[20:23], s47 offen nt\n"
        "s_barrier\n"
        "s_memrealtime s[6:7]\n"
        "s_waitcnt lgkmcnt(0)\n"
        "s_sub_u32 s6, s6, s42\n"
        "s_cmpk_gt_u32 s6, 265\n"
        "s_cbranch_scc1 .Lk_B1a\n"
        "s_sleep 9\n"
        ".Lk_B1a:\n"
        "buffer_load_dwordx2 v[2:3], v5, s[28:31], s59 offen sc1\n"
        "buffer_load_dwordx4 v[20:23], v4, s[20:23], s47 offen offset:2048 nt\n"
        "s_addk_i32 s47, -0x1000\n"
        "buffer_load_dwordx4 v[24:27], v6, s[20:23], s47 offen nt\n"
        "buffer_load_dwordx4 v[28:31], v6, s[20:23], s47 offen offset:2048 nt\n"
        "s_addk_i32 s47, 0x1000\n"
        "buffer_load_dwordx4 v[32:35], v6, s[20:23], s47 offen nt\n"
        "buffer_load_dwordx4 v[36:39], v6, s[20:23], s47 offen offset:2048 nt\n"
        "s_barrier\n"
        "s_waitcnt vmcnt(5)\n"
        ".Lk_B2:\n"
        "v_cmp_ne_u32 vcc, s8, v2\n"
        "v_min_u32_dpp v3, v3, v3 quad_perm:[2,3,0,1] row_mask:0xf bank_mask:0xf\n"
        "s_cbranch_vccnz .Lk_B7\n"
        ".Lk_B8:\n"
        "s_nop 0\n"
        "v_mov_b32_dpp v2, v3 quad_perm:[1,1,1,1] row_mask:0xf bank_mask:0xf\n"
        "v_sub_u32_dpp v2, v3, v2 quad_perm:[0,0,0,0] row_mask:0xf bank_mask:0xf\n"
        "v_ashrrev_i32 v2, 31, v2\n"
        "v_xor_b32 v2, s58, v2\n"
        "v_and_b32 v2, s19, v2\n"
        "v_mov_b32 v3, s30\n"
        "ds_write_b64 v127, v[2:3] offset:32768\n"
        "v_add_u32 v5, v2, v7\n"
        "v_xor_b32 v117, v117, v2\n"
        "v_perm_b32 v44, v12, v8, s44\n"
        "v_perm_b32 v48, v12, v8, s45\n"
        "v_perm_b32 v52, v13, v9, s44\n"
        "v_perm_b32 v56, v13, v9, s45\n"
        "v_perm_b32 v60, v14, v10, s44\n"
        "v_perm_b32 v64, v14, v10, s45\n"
        "v_perm_b32 v68, v15, v11, s44\n"
        "v_perm_b32 v72, v15, v11, s45\n"
        "s_waitcnt vmcnt(4)\n"
        "v_perm_b32 v45, v20, v16, s44\n"
        "v_perm_b32 v49, v20, v16, s45\n"
        "v_perm_b32 v53, v21, v17, s44\n"
        "v_perm_b32 v57, v21, v17, s45\n"
        "v_perm_b32 v61, v22, v18, s44\n"
        "v_perm_b32 v65, v22, v18, s45\n"
        "v_perm_b32 v69, v23, v19, s44\n"
        "v_perm_b32 v73, v23, v19, s45\n"
        "s_waitcnt vmcnt(2)\n"
        "v_perm_b32 v46, v28, v24, s44\n"
        "v_perm_b32 v50, v28, v24, s45\n"
        "v_perm_b32 v54, v29, v25, s44\n"
        "v_perm_b32 v58, v29, v25, s45\n"
        "v_perm_b32 v62, v30, v26, s44\n"
        "v_perm_b32 v66, v30, v26, s45\n"
        "v_perm_b32 v70, v31, v27, s44\n"
        "v_perm_b32 v74, v31, v27, s45\n"
        "s_waitcnt vmcnt(0)\n"
        ".Lk_B3:\n"
        "s_cmp_lg_u32 s46, 3\n"
        "s_cbranch_scc1 .Lk_B3a\n"
        "buffer_load_dwordx4 v[92:95], v5, s[28:31], s48 offen sc1\n"
        "buffer_load_dwordx4 v[96:99], v5, s[28:31], s48 offen offset:2048 sc1\n"
        "buffer_load_dwordx4 v[100:103], v5, s[28:31], s49 offen sc1\n"
        "buffer_load_dwordx4 v[104:107], v5, s[28:31], s49 offen offset:2048 sc1\n"
        ".Lk_B3a:\n"
        "buffer_load_dwordx4 v[76:79], v5, s[28:31], s50 offen sc1\n"
        "buffer_load_dwordx4 v[80:83], v5, s[28:31], s50 offen offset:2048 sc1\n"
        "buffer_load_dwordx4 v[84:87], v5, s[28:31], s51 offen sc1\n"
        "buffer_load_dwordx4 v[88:91], v5, s[28:31], s51 offen offset:2048 sc1\n"
        ".Lk_B3e:\n"
        "v_perm_b32 v47, v36, v32, s44\n"
        "v_perm_b32 v51, v36, v32, s45\n"
        "v_perm_b32 v55, v37, v33, s44\n"
        "v_perm_b32 v59, v37, v33, s45\n"
        "v_perm_b32 v63, v38, v34, s44\n"
        "v_perm_b32 v67, v38, v34, s45\n"
        "v_perm_b32 v71, v39, v35, s44\n"
        "v_perm_b32 v75, v39, v35, s45\n"
        "v_mov_b32 v22, 0\n"
        "s_cbranch_scc1 .Lk_B3b\n"
        "s_waitcnt vmcnt(4)\n"
        "v_xor_b32 v92, s9, v92\n"
        "v_xor_b32 v93, s10, v93\n"
        "v_xor_b32 v94, s11, v94\n"
        "v_xor_b32 v95, s12, v95\n"
        "v_or3_b32 v22, v92, v93, v22\n"
        "v_or3_b32 v22, v94, v95, v22\n"
        "v_xor_b32 v96, s9, v96\n"
        "v_xor_b32 v97, s10, v97\n"
        "v_xor_b32 v98, s11, v98\n"
        "v_xor_b32 v99, s12, v99\n"
        "v_or3_b32 v22, v96, v97, v22\n"
        "v_or3_b32 v22, v98, v99, v22\n"
        "v_xor_b32 v100, s9, v100\n"
        "v_xor_b32 v101, s10, v101\n"
        "v_xor_b32 v102, s11, v102\n"
        "v_xor_b32 v103, s12, v103\n"
        "v_or3_b32 v22, v100, v101, v22\n"
        "v_or3_b32 v22, v102, v103, v22\n"
        "v_xor_b32 v104, s9, v104\n"
        "v_xor_b32 v105, s10, v105\n"
        "v_xor_b32 v106, s11, v106\n"
        "v_xor_b32 v107, s12, v107\n"
        "v_or3_b32 v22, v104, v105, v22\n"
        "v_or3_b32 v22, v106, v107, v22\n"
        "v_pk_add_f32 v[92:93], v[92:93], v[96:97]\n"
        "v_pk_add_f32 v[94:95], v[94:95], v[98:99]\n"
        "v_pk_add_f32 v[100:101], v[100:101], v[104:105]\n"
        "v_pk_add_f32 v[102:103], v[102:103], v[106:107]\n"
        ".Lk_B3b:\n"
        "s_waitcnt vmcnt(3)\n"
        "v_xor_b32 v76, s9, v76\n"
        "v_xor_b32 v77, s10, v77\n"
        "v_xor_b32 v78, s11, v78\n"
        "v_xor_b32 v79, s12, v79\n"
        "s_waitcnt vmcnt(2)\n"
        "v_xor_b32 v80, s9, v80\n"
        "v_xor_b32 v81, s10, v81\n"
        "v_xor_b32 v82, s11, v82\n"
        "v_xor_b32 v83, s12, v83\n"
        "v_pk_add_f32 v[8:9], v[76:77], v[80:81]\n"
        "v_pk_add_f32 v[10:11], v[78:79], v[82:83]\n"
        "v_pk_add_f32 v[8:9], v[8:9], v[92:93]\n"
        "v_pk_add_f32 v[10:11], v[10:11], v[94:95]\n"
        ".Lk_B3c:\n"
        "v_pk_mul_f32 v[12:13], v[8:9], s[56:57] op_sel_hi:[1,0]\n"
        "v_pk_mul_f32 v[14:15], v[10:11], s[56:57] op_sel_hi:[1,0]\n"
        "v_exp_f32 v12, v12\n"
        "v_exp_f32 v13, v13\n"
        "v_exp_f32 v14, v14\n"
        "v_exp_f32 v15, v15\n"
        "v_pk_add_f32 v[12:13], v[12:13], 1.0 op_sel_hi:[1,0]\n"
        "v_pk_add_f32 v[14:15], v[14:15], 1.0 op_sel_hi:[1,0]\n"
        "v_rcp_f32 v12, v12\n"
        "v_rcp_f32 v13, v13\n"
        "v_rcp_f32 v14, v14\n"
        "v_rcp_f32 v15, v15\n"
        "s_waitcnt vmcnt(0)\n"
        "v_xor_b32 v84, s9, v84\n"
        "v_xor_b32 v85, s10, v85\n"
        "v_xor_b32 v86, s11, v86\n"
        "v_xor_b32 v87, s12, v87\n"
        "v_xor_b32 v88, s9, v88\n"
        "v_xor_b32 v89, s10, v89\n"
        "v_xor_b32 v90, s11, v90\n"
        "v_xor_b32 v91, s12, v91\n"
        "v_pk_add_f32 v[16:17], v[84:85], v[88:89]\n"
        "v_pk_add_f32 v[18:19], v[86:87], v[90:91]\n"
        "v_pk_mul_f32 v[8:9], v[8:9], v[12:13]\n"
        "v_pk_mul_f32 v[10:11], v[10:11], v[14:15]\n"
        "v_pk_add_f32 v[16:17], v[16:17], v[100:101]\n"
        "v_pk_add_f32 v[18:19], v[18:19], v[102:103]\n"
        ".Lk_B3d:\n"
        "v_pk_mul_f32 v[8:9], v[8:9], v[16:17]\n"
        "v_pk_mul_f32 v[10:11], v[10:11], v[18:19]\n"
        "v_cvt_pk_bf16_f32 v20, v8, v9\n"
        "v_cvt_pk_bf16_f32 v21, v10, v11\n"
        "v_or3_b32 v22, v76, v77, v22\n"
        "v_mov_b32_dpp v108, v20 quad_perm:[0,1,2,3] row_mask:0xf bank_mask:0x3\n"
        "v_mov_b32_dpp v109, v21 quad_perm:[0,1,2,3] row_mask:0xf bank_mask:0x3\n"
        "v_mov_b32_dpp v110, v20 row_ror:8 row_mask:0xf bank_mask:0x3\n"
        "v_mov_b32_dpp v111, v21 row_ror:8 row_mask:0xf bank_mask:0x3\n"
        "v_mov_b32_dpp v112, v20 row_ror:8 row_mask:0xf bank_mask:0xc\n"
        "v_mov_b32_dpp v113, v21 row_ror:8 row_mask:0xf bank_mask:0xc\n"
        "v_mfma_f32_16x16x32_bf16 v[24:27], v[108:111], v[44:47], 0\n"
        "v_mov_b32_dpp v114, v20 quad_perm:[0,1,2,3] row_mask:0xf bank_mask:0xc\n"
        "v_mov_b32_dpp v115, v21 quad_perm:[0,1,2,3] row_mask:0xf bank_mask:0xc\n"
        "v_mfma_f32_16x16x32_bf16 v[28:31], v[108:111], v[48:51], 0\n"
        "v_or3_b32 v22, v78, v79, v22\n"
        "v_or3_b32 v22, v80, v81, v22\n"
        "v_mfma_f32_16x16x32_bf16 v[24:27], v[112:115], v[60:63], v[24:27]\n"
        "v_or3_b32 v22, v82, v83, v22\n"
        "v_or3_b32 v22, v84, v85, v22\n"
        "v_mfma_f32_16x16x32_bf16 v[28:31], v[112:115], v[64:67], v[28:31]\n"
        "v_or3_b32 v22, v86, v87, v22\n"
        "v_or3_b32 v22, v88, v89, v22\n"
        "v_mfma_f32_16x16x32_bf16 v[40:43], v[108:111], v[52:55], 0\n"
        "v_or3_b32 v22, v90, v91, v22\n"
        "v_and_b32 v22, 0xff, v22\n"
        "v_mfma_f32_16x16x32_bf16 v[40:43], v[112:115], v[68:71], v[40:43]\n"
        "v_cmp_ne_u32 vcc, 0, v22\n"
        "ds_write_b128 v120, v[24:27] offset:16384\n"
        "v_mfma_f32_16x16x32_bf16 v[124:127], v[108:111], v[56:59], 0\n"
        "ds_write_b128 v121, v[28:31] offset:16384\n"
        "v_mfma_f32_16x16x32_bf16 v[124:127], v[112:115], v[72:75], v[124:127]\n"
        "s_cbranch_vccnz .Lk_B5\n"
        ".Lk_B4:\n"
        "s_nop 1\n"
        "ds_write_b128 v122, v[40:43] offset:16384\n"
        "s_nop 3\n"
        "ds_write_b128 v123, v[124:127] offset:16384\n"
        "s_waitcnt lgkmcnt(0)\n"
        "s_barrier\n"
        "ds_read_b32 v2, v119 offset:32776\n"
        "ds_read_b128 v[8:11], v116 offset:16384\n"
        "ds_read_b128 v[12:15], v116 offset:20480\n"
        "ds_read_b128 v[16:19], v116 offset:24576\n"
        "ds_read_b128 v[20:23], v116 offset:28672\n"
        "s_waitcnt lgkmcnt(2)\n"
        "v_xor_b32 v117, v117, v2\n"
        "v_pk_add_f32 v[8:9], v[8:9], v[12:13]\n"
        "v_pk_add_f32 v[10:11], v[10:11], v[14:15]\n"
        "s_waitcnt lgkmcnt(0)\n"
        "v_pk_add_f32 v[16:17], v[16:17], v[20:21]\n"
        "v_pk_add_f32 v[18:19], v[18:19], v[22:23]\n"
        "v_pk_add_f32 v[8:9], v[8:9], v[16:17]\n"
        "v_pk_add_f32 v[10:11], v[10:11], v[18:19]\n"
        "v_and_or_b32 v8, v8, v118, s9\n"
        "v_and_or_b32 v9, v9, v118, s10\n"
        "v_and_or_b32 v10, v10, v118, s11\n"
        "v_and_or_b32 v11, v11, v118, s12\n"
        "buffer_store_dwordx4 v[8:11], v117, s[28:31], s53 offen sc1\n"
        "s_endpgm\n"
        :
        : [ka] "s"(__builtin_amdgcn_kernarg_segment_ptr()), [bid] "s"(blockIdx.x ^ 5u), [tid] "{v0}"(threadIdx.x)
        : "v1", "v2", "v3", "v4", "v5", "v6", "v7", "v8", "v9", "v10", "v11", "v12", "v13", "v14", "v15", "v16", "v17", "v18", "v19", "v20", "v21", "v22", "v23", "v24", "v25", "v26", "v27", "v28", "v29", "v30", "v31", "v32", "v33", "v34", "v35", "v36", "v37", "v38", "v39", "v40", "v41", "v42", "v43", "v44", "v45", "v46", "v47", "v48", "v49", "v50", "v51", "v52", "v53", "v54", "v55", "v56", "v57", "v58", "v59", "v60", "v61", "v62", "v63", "v64", "v65", "v66", "v67", "v68", "v69", "v70", "v71", "v72", "v73", "v74", "v75", "v76", "v77", "v78", "v79", "v80", "v81", "v82", "v83", "v84", "v85", "v86", "v87", "v88", "v89", "v90", "v91", "v92", "v93", "v94", "v95", "v96", "v97", "v98", "v99", "v100", "v101", "v102", "v103", "v104", "v105", "v106", "v107", "v108", "v109", "v110", "v111", "v112", "v113", "v114", "v115", "v116", "v117", "v118", "v119", "v120", "v121", "v122", "v123", "v124", "v125", "v126", "v127", "s4", "s5", "s6", "s7", "s8", "s9", "s10", "s11", "s12", "s13", "s14", "s15", "s16", "s17", "s18", "s19", "s20", "s21", "s22", "s23", "s24", "s25", "s26", "s27", "s28", "s29", "s30", "s31", "s34", "s35", "s36", "s37", "s38", "s39", "s40", "s41", "s42", "s43", "s44", "s45", "s46", "s47", "s48", "s49", "s50", "s51", "s52", "s53", "s54", "s55", "s56", "s57", "s58", "s59", "s60", "s61", "s62", "s63", "s64", "s65", "s66", "s67", "s68", "s69", "s70", "s71", "s72", "s73", "s74", "s75", "s76", "s77", "s78", "s79", "vcc", "scc", "memory");
    __builtin_unreachable();
}

extern "C" void kernel_launch(void* const* d_in, const int* in_sizes, int n_in, void* d_out, int out_size, void* d_ws, size_t ws_size,
                              hipStream_t stream) {
    swiglu_k<<<256, 1024, 33 * 1024, stream>>>(d_in[0], d_in[1], d_in[2], d_in[3], d_out, d_ws, tab_k);
}
```
